```python
import torch

def _softmax(x, axis=-1):
    return torch.softmax(x, dim=axis)
def _gelu(x):
    return torch.nn.functional.gelu(x, approximate="tanh")
def _silu(x):
    return torch.nn.functional.silu(x)
def _sigmoid(x):
    return torch.sigmoid(x)
def _layernorm(x, axis=-1, eps=1e-5):
    m = x.mean(dim=axis, keepdim=True)
    v = x.var(dim=axis, keepdim=True, unbiased=False)
    return (x - m) / torch.sqrt(v + eps)
def _scatter_add(tgt, idx, src, axis=0):
    return tgt.clone().index_add_(axis, idx, src)
def _segment_sum(src, seg, n):
    out = torch.zeros((n,) + src.shape[1:], dtype=src.dtype)
    return out.index_add_(0, seg, src)
def _pad(x, pw):
    flat = tuple(v for pair in reversed(pw) for v in pair)
    return torch.nn.functional.pad(x, flat)

kernel_name = 'gated_mix__grouped_kv_mqa__sliding_local_dilated__prompt'

def setup_inputs():

    _w = (lambda a: (lambda t: t.to(torch.bfloat16) if t.is_floating_point() else t)(torch.as_tensor(a)))
    def _A_setup():
        B, S, D, H = 1, 2048, 640, 32
        Dh = D // H
        return {
            "x": _w((lambda *s: torch.randn(*s, dtype=torch.bfloat16) * 0.05)(B, S, D)),
            "wq": _w((lambda *s: torch.randn(*s, dtype=torch.bfloat16) * 1.6)(D, H * Dh)),
            "wk": _w((lambda *s: torch.randn(*s, dtype=torch.bfloat16) * 1.6)(D, Dh)),
            "wv": _w((lambda *s: torch.randn(*s, dtype=torch.bfloat16) * 0.2)(D, Dh)),
            "wo": _w((lambda *s: torch.randn(*s, dtype=torch.bfloat16) * 0.2)(H * Dh, D)),
        }
    _A_in = _A_setup()

    def _B_setup():
        B, S, D = 1, 2048, 640
        return {
            "x": _w((lambda *s: torch.randn(*s, dtype=torch.bfloat16) * 0.05)(B, S, D)),
            "wqkv": _w((lambda *s: torch.randn(*s, dtype=torch.bfloat16) * 0.8)(D, 3 * D)),
            "wo": _w((lambda *s: torch.randn(*s, dtype=torch.bfloat16) * 0.05)(D, D)),
        }
    _B_in = _B_setup()

    return {
        "A_x": _A_in["x"],
        "A_wq": _A_in["wq"],
        "A_wk": _A_in["wk"],
        "A_wv": _A_in["wv"],
        "A_wo": _A_in["wo"],
        "B_x": _B_in["x"],
        "B_wqkv": _B_in["wqkv"],
        "B_wo": _B_in["wo"],
        "gate_w": _w((lambda *s: torch.randn(*s, dtype=torch.bfloat16) * 0.05)(640) * 16.0),
    }

def reference(A_x, A_wq, A_wk, A_wv, A_wo, B_x, B_wqkv, B_wo, gate_w):
    def _A_ref(x, wq, wk, wv, wo):
        B, S, D = x.shape
        H = 32
        Dh = D // H
        q = (lambda x, *d: x.permute(*d))((x @ wq).reshape(B, S, H, Dh), 0, 2, 1, 3)
        k = x @ wk
        v = x @ wv
        scores = torch.einsum("bhqd,bkd->bhqk", q, k) * (Dh ** -0.5)
        p = _softmax(scores, axis=-1)
        o = torch.einsum("bhqk,bkd->bhqd", p, v)
        return (lambda x, *d: x.permute(*d))(o, 0, 2, 1, 3).reshape(B, S, H * Dh) @ wo

    def _B_ref(x, wqkv, wo):
        B, S, D = x.shape
        H = 32; Dh = D // H
        W, dil = 8, 2
        qkv = (lambda x, *d: x.permute(*d))((x @ wqkv).reshape(B, S, 3, H, Dh), 2, 0, 3, 1, 4)
        q, k, v = qkv[0], qkv[1], qkv[2]
        scale = Dh ** -0.5
        pos = torch.arange(S)
        off = torch.arange(W) * dil
        idx = pos[:, None] - off[None, :]
        mask = idx >= 0
        idx_c = torch.where(mask, idx, 0).reshape(-1)
        kg = k[:, :, idx_c, :].reshape(B, H, S, W, Dh)
        vg = v[:, :, idx_c, :].reshape(B, H, S, W, Dh)
        logits = torch.einsum("bhsd,bhswd->bhsw", q, kg) * scale
        logits = torch.where(mask[None, None, :, :], logits, -1e9)
        attn = _softmax(logits, axis=-1)
        o = torch.einsum("bhsw,bhswd->bhsd", attn, vg)
        return (lambda x, *d: x.permute(*d))(o, 0, 2, 1, 3).reshape(B, S, D) @ wo

    x = A_x
    g = _sigmoid((x * gate_w).sum(-1, keepdims=True))
    a = _A_ref(x, A_wq, A_wk, A_wv, A_wo)
    b = _B_ref(x, B_wqkv, B_wo)
    return g * a + (1 - g) * b
```

```cpp
#include <hip/hip_runtime.h>
#include <hip/hip_bf16.h>
#include <stdint.h>
typedef unsigned short u16;
__device__ __forceinline__ float b2f(u16 b) { return __uint_as_float((unsigned)b << 16); }
__device__ __forceinline__ u16 f2b(float f) { unsigned u = __float_as_uint(f); u += 0x7fffu + ((u >> 16) & 1u); return (u16)(u >> 16); }
__device__ __forceinline__ float rb(float f) { return b2f(f2b(f)); }
__global__ void n_gemm(const u16* A, const u16* W, float* C, int M, int N, int K) {
  int n = blockIdx.x * blockDim.x + threadIdx.x, m = blockIdx.y; if (n >= N) return;
  float s = 0; for (int k = 0; k < K; ++k) s += b2f(A[m * K + k]) * b2f(W[k * N + n]);
  C[m * N + n] = rb(s);
}
__global__ void n_attnA(const float* q, const float* k, const float* v, u16* o) {
  int i = blockIdx.x * blockDim.x + threadIdx.x; int t = i >> 5, h = i & 31;
  float qq[20]; for (int d = 0; d < 20; ++d) qq[d] = q[t * 640 + h * 20 + d];
  const float sc = rb(0.22360679774997896f);
  float m = -1e30f;
  for (int j = 0; j < 2048; ++j) { float s = 0; for (int d = 0; d < 20; ++d) s += qq[d] * k[j * 20 + d]; s = rb(rb(s) * 0.22360679774997896f); m = fmaxf(m, s); }
  float l = 0; for (int j = 0; j < 2048; ++j) { float s = 0; for (int d = 0; d < 20; ++d) s += qq[d] * k[j * 20 + d]; s = rb(rb(s) * 0.22360679774997896f); l += __expf(s - m); }
  float acc[20]; for (int d = 0; d < 20; ++d) acc[d] = 0;
  for (int j = 0; j < 2048; ++j) { float s = 0; for (int d = 0; d < 20; ++d) s += qq[d] * k[j * 20 + d]; s = rb(rb(s) * 0.22360679774997896f); float p = rb(__expf(s - m) / l); for (int d = 0; d < 20; ++d) acc[d] += p * v[j * 20 + d]; }
  for (int d = 0; d < 20; ++d) o[t * 640 + h * 20 + d] = f2b(acc[d]);
  (void)sc;
}
__global__ void n_attnB(const float* qkv, u16* o) {
  int i = blockIdx.x * blockDim.x + threadIdx.x; int t = i >> 5, h = i & 31;
  float lg[8]; float m = -1e30f;
  for (int w = 0; w < 8; ++w) { int j = t - 2 * w; float s = 0; if (j >= 0) { for (int d = 0; d < 20; ++d) s += qkv[t * 1920 + h * 20 + d] * qkv[j * 1920 + 640 + h * 20 + d]; s = rb(rb(s) * 0.22360679774997896f); } else s = rb(-1e9f); lg[w] = s; m = fmaxf(m, s); }
  float l = 0; for (int w = 0; w < 8; ++w) { lg[w] = __expf(lg[w] - m); l += lg[w]; }
  for (int d = 0; d < 20; ++d) { float a = 0; for (int w = 0; w < 8; ++w) { int j = max(t - 2 * w, 0); a += rb(lg[w] / l) * qkv[j * 1920 + 1280 + h * 20 + d]; } o[t * 640 + h * 20 + d] = f2b(a); }
}
__global__ void n_mix(const u16* x, const u16* gw, const float* a, const float* b, u16* out) {
  int t = blockIdx.x; __shared__ float g;
  if (threadIdx.x == 0) { float s = 0; for (int k = 0; k < 640; ++k) s += rb(b2f(x[t * 640 + k]) * b2f(gw[k])); s = rb(s); g = rb(1.f / (1.f + __expf(-s))); }
  __syncthreads();
  for (int n = threadIdx.x; n < 640; n += blockDim.x) out[t * 640 + n] = f2b(rb(g * a[t * 640 + n]) + rb(rb(1.f - g) * b[t * 640 + n]));
}
#define NAIVE_NAME kernel_launch
extern "C" void NAIVE_NAME(void* const* d_in, const int* in_sizes, int n_in, void* d_out, int out_size, void* d_ws, size_t ws_size, hipStream_t stream) {
  const u16 *x = (const u16*)d_in[0], *wq = (const u16*)d_in[1], *wk = (const u16*)d_in[2], *wv = (const u16*)d_in[3], *woA = (const u16*)d_in[4], *wqkv = (const u16*)d_in[6], *woB = (const u16*)d_in[7], *gw = (const u16*)d_in[8];
  float* ws = (float*)d_ws;
  float *q = ws, *k = q + 2048 * 640, *v = k + 2048 * 20, *qkv = v + 2048 * 20, *a = qkv + 2048 * 1920, *b = a + 2048 * 640;
  u16 *oA = (u16*)(b + 2048 * 640), *oB = oA + 2048 * 640;
  n_gemm<<<dim3(5, 2048), 128, 0, stream>>>(x, wq, q, 2048, 640, 640);
  n_gemm<<<dim3(1, 2048), 64, 0, stream>>>(x, wk, k, 2048, 20, 640);
  n_gemm<<<dim3(1, 2048), 64, 0, stream>>>(x, wv, v, 2048, 20, 640);
  n_gemm<<<dim3(15, 2048), 128, 0, stream>>>(x, wqkv, qkv, 2048, 1920, 640);
  n_attnA<<<1024, 64, 0, stream>>>(q, k, v, oA);
  n_attnB<<<1024, 64, 0, stream>>>(qkv, oB);
  n_gemm<<<dim3(5, 2048), 128, 0, stream>>>(oA, woA, a, 2048, 640, 640);
  n_gemm<<<dim3(5, 2048), 128, 0, stream>>>(oB, woB, b, 2048, 640, 640);
  n_mix<<<2048, 128, 0, stream>>>(x, gw, a, b, (u16*)d_out);
}
```

```cpp
#include <hip/hip_runtime.h>
#include <hip/hip_bf16.h>
#include <stdint.h>

typedef unsigned short u16;
using bf16x8 = __attribute__((ext_vector_type(8))) __bf16;
using bf16x16 = __attribute__((ext_vector_type(16))) __bf16;
using bf16x2 = __attribute__((ext_vector_type(2))) __bf16;
using s16x4 = __attribute__((ext_vector_type(4))) short;
using f32x16 = __attribute__((ext_vector_type(16))) float;
using f32x8 = __attribute__((ext_vector_type(8))) float;
using f32x4 = __attribute__((ext_vector_type(4))) float;
using f32x2 = __attribute__((ext_vector_type(2))) float;
using u32x4 = __attribute__((ext_vector_type(4))) unsigned;
using u32x2 = __attribute__((ext_vector_type(2))) unsigned;
#define LDSP __attribute__((address_space(3)))
#define DI __device__ __forceinline__
template <bool B> struct BoolTag { static constexpr bool value = B; };
#define SBAR() __builtin_amdgcn_sched_barrier(0)

constexpr int DM = 640;
constexpr float QSCALE = 0.22360679774997896f * 1.4426950408889634f;
constexpr int WP_HG = 204800;
constexpr size_t WS_WP = 0, WS_XI = 3276800, WS_WO = WS_XI + 2621440, WS_KV = WS_WO + 1638400, WS_G = WS_KV + 180224, WS_FL = WS_G + 8192, WS_F1 = WS_FL + 4096, WS_OI = WS_F1 + 32768,
                 WS_END = WS_OI + 5242880;
static_assert(WS_END < (64u << 20), "workspace");
constexpr int TILE_B = 2432, OFF_V = 1024, VB = 704, OFF_K1 = 4 * TILE_B, CH_B = 11264;
constexpr int STG_B = 20480, L_HX = 5 * STG_B, L_KVR = 6 * STG_B, L_BS = 0, BS_ROW = 304, L_G = L_KVR + 3 * CH_B, LDS_MAIN = 163840;

DI unsigned cvtpk(float lo, float hi) { bf16x2 r = {(__bf16)lo, (__bf16)hi}; return __builtin_bit_cast(unsigned, r); }
DI s16x4 trread(const LDSP char* p) { return __builtin_amdgcn_ds_read_tr16_b64_v4i16((LDSP s16x4*)p); }
DI bf16x8 trfrag(const LDSP char* p, int hi_off) { s16x4 a = trread(p), b = trread(p + hi_off); return __builtin_bit_cast(bf16x8, __builtin_shufflevector(a, b, 0, 1, 2, 3, 4, 5, 6, 7)); }
#define MFMA32(a, b, c) __builtin_amdgcn_mfma_f32_32x32x16_bf16(a, b, c, 0, 0, 0)
#define MFMA32K8(a, b, c) __builtin_amdgcn_mfma_f32_32x32x8bf16_1k(a, b, c, 0, 0, 0)
#define MFMA16(a, b, c) __builtin_amdgcn_mfma_f32_16x16x32_bf16(a, b, c, 0, 0, 0)
#define SMFMAC(a, b, c) __builtin_amdgcn_smfmac_f32_32x32x32_bf16(a, __builtin_bit_cast(bf16x16, b), c, 0xDDDD, 0, 0)
template <int IMM> DI void glds(const void* base, unsigned voff, unsigned m0v) {
  unsigned sv;
  asm volatile("s_mov_b32 %0, m0\n\ts_mov_b32 m0, %3\n\ts_nop 0\n\tglobal_load_lds_dwordx4 %1, %2 offset:%4 sc1\n\ts_mov_b32 m0, %0"
               : "=&s"(sv) : "v"(voff), "s"(base), "s"(m0v), "i"(IMM) : "memory");
}
#define GL(v, s, o) "global_load_lds_dwordx4 " v ", " s " offset:" #o " sc1\n\t"
#define M0ADD "s_add_u32 m0, m0, 0x1000\n\ts_nop 0\n\t"
DI void stage_w(const char* src, unsigned lo, unsigned m0v) {
  unsigned sv; const char* s1 = src + 4096;
  asm volatile("s_mov_b32 %0, m0\n\ts_mov_b32 m0, %4\n\ts_nop 0\n\t" GL("%1", "%2", 0) GL("%1", "%2", 1024) GL("%1", "%2", 2048) GL("%1", "%2", 3072) M0ADD
               GL("%1", "%3", 0) "s_mov_b32 m0, %0" : "=&s"(sv) : "v"(lo), "s"(src), "s"(s1), "s"(m0v) : "memory", "scc");
}
typedef __amdgpu_buffer_rsrc_t rsrc_t;
DI rsrc_t mkrsrc(const void* p, unsigned bytes) { return __builtin_amdgcn_make_buffer_rsrc((void*)p, 0, bytes, 0x00020000); }
DI void st16(rsrc_t r, unsigned off, u32x4 v) { __builtin_amdgcn_raw_buffer_store_b128(v, r, off, 0, 16); }
DI void st8(rsrc_t r, unsigned off, u32x2 v) { __builtin_amdgcn_raw_buffer_store_b64(v, r, off, 0, 16); }
DI u32x2 ld8(rsrc_t r, unsigned off) { return __builtin_amdgcn_raw_buffer_load_b64(r, off, 0, 16); }
DI void st4(rsrc_t r, unsigned off, unsigned v) { __builtin_amdgcn_raw_buffer_store_b32(v, r, off, 0, 16); }
DI u32x4 ld16(rsrc_t r, unsigned off, unsigned soff) { return __builtin_amdgcn_raw_buffer_load_b128(r, off, soff, 16); }
DI unsigned ld4(rsrc_t r, unsigned off) { return __builtin_amdgcn_raw_buffer_load_b32(r, off, 0, 16); }
DI u32x4 nosent(u32x4 v) { return __builtin_elementwise_min(v, (u32x4)(0xFFFFFFFEu)); }
constexpr unsigned SPIN_CAP = 1u << 16;
#define WAITVM(n) asm volatile("s_waitcnt vmcnt(%0)" ::"i"(n) : "memory")
#define BARRIER() asm volatile("s_waitcnt lgkmcnt(0)\n\ts_barrier" ::: "memory")
DI void block_map(int bid, int& tg, int& hg) { const int xcd = bid & 7, j = bid >> 3; tg = (xcd >> 1) * 4 + (j >> 3); hg = (xcd & 1) * 8 + (j & 7); }
DI float xhalf(float v) {
  auto r = __builtin_amdgcn_permlane32_swap(__float_as_uint(v), __float_as_uint(v), false, false);
  return __uint_as_float((threadIdx.x & 32) ? r[0] : r[1]);
}

DI void wo_fetch(const u16* __restrict__ woA, const u16* __restrict__ woB, char* lw, int u, int lane) {
  const int ch = u >> 2, br = ch >= 80, cg = ch - br * 80, pr = cg / 5, c5 = cg - pr * 5;
  const int r0 = pr * 40 + (c5 < 4 ? 20 * (c5 >> 1) + 4 * (c5 & 1) : 16), dr = c5 < 4 ? 4 : 16;
  const char* src = (const char*)(br ? woB : woA) + r0 * 1280 + (u & 3) * 320;
#pragma unroll
  for (int i = 0; i < 3; ++i) {
    const int e = min(i * 64 + lane, 159), r = e / 20;
    unsigned sv;
    asm volatile("s_mov_b32 %0, m0\n\ts_mov_b32 m0, %3\n\ts_nop 0\n\tglobal_load_lds_dwordx4 %1, %2 nt\n\ts_mov_b32 m0, %0"
                 : "=&s"(sv) : "v"((r + (r >= 4 ? dr : 0)) * 1280 + (e - r * 20) * 16), "s"(src), "s"((unsigned)(uintptr_t)lw + i * 1024) : "memory");
  }
}
DI void wo_put(rsrc_t wsr, char* lw, int u, int lane) {
  const int ch = u >> 2, br = ch >= 80, cg = ch - br * 80;
#pragma unroll
  for (int i = 0; i < 3; ++i) {
    bf16x8 f = trfrag((const LDSP char*)lw + ((lane & 15) >> 2) * 320 + min(i * 16 + (lane >> 4) * 4 + (lane & 3), 39) * 8, 1280);
    asm volatile("" : "+v"(f));
    const int t16 = i * 4 + (lane >> 4);
    if (t16 < 10) st16(wsr, WS_WO + (((u & 3) * 2 + (t16 >= 5)) * 2 + br) * 102400 + (cg >> 2) * 5120 + (t16 - (t16 >= 5) * 5) * 1024 + (cg & 3) * 256 + (lane & 15) * 16, __builtin_bit_cast(u32x4, f));
  }
}
DI void repack_wo(const u16* woA, const u16* woB, rsrc_t wsr, char* lw, int u, int lane) { wo_fetch(woA, woB, lw, u, lane); WAITVM(0); wo_put(wsr, lw, u, lane); }
DI void phase0(const u16* __restrict__ x, const u16* __restrict__ wq, const u16* __restrict__ wk, const u16* __restrict__ wv, const u16* __restrict__ wqkv,
               const u16* __restrict__ gw, rsrc_t wsr, char* lds, int tg, int hg, int wid, int lane, unsigned& epv, volatile LDSP unsigned* arrive) {
  if (wid < 5) {
    const int r8 = tg * 5 + wid, m = hg & 3;
    char* lw = lds - L_KVR + wid * 3072;
    const unsigned stride = m == 0 ? 1280u : 3840u;
    const char* src = (m == 0 ? (const char*)wq : (const char*)wqkv + (m - 1) * 1280) + r8 * 8 * stride + (hg >> 2) * 320;
#pragma unroll
    for (int i = 0; i < 3; ++i) {
      const int e = min(i * 64 + lane, 159), r = e / 20;
      unsigned sv;
      asm volatile("s_mov_b32 %0, m0\n\ts_mov_b32 m0, %3\n\ts_nop 0\n\tglobal_load_lds_dwordx4 %1, %2 nt\n\ts_mov_b32 m0, %0"
                   : "=&s"(sv) : "v"(r * stride + (e - r * 20) * 16), "s"(src), "s"((unsigned)(uintptr_t)lw + i * 1024) : "memory");
    }
    epv = ld4(wsr, wid == 4 ? WS_FL + 3072 : 0x7FFFFFF0u);
    if (wid == 4 && lane < 24) arrive[lane] = 0u;
    BARRIER();
    int ra[3]; unsigned dst[3];
#pragma unroll
    for (int i = 0; i < 3; ++i) {
      { const int D = min(i * 16 + (lane >> 4) * 4 + (lane & 3), 39), pr = D / 10, g = D - pr * 10;
        ra[i] = ((lane & 15) >> 2) * 320 + pr * 80 + (m == 0 ? g * 8 : (g & 1) * 40 + (g >> 1) * 8); }
      const int c = min(i * 64 + lane, 159), pr = c / 40, cc = c - pr * 40, gi = cc >> 3, hh = (cc >> 2) & 1;
      int pos;
      if (m == 0) pos = cc + (cc >= 20 ? 12 : 0);
      else if (m == 1) pos = (gi == 0 ? 20 + hh * 32 : ((0x48403818u >> (gi * 8 - 8)) & 255) + hh * 4) + (cc & 3);
      else pos = m * 40 + cc;
      dst[i] = WS_WP + ((hg & 12) + pr) * WP_HG + (r8 >> 1) * 5120 + (pos >> 5) * 1024 + ((r8 & 1) * 32 + (pos & 31)) * 16;
    }
    WAITVM(1);
#pragma unroll
    for (int i = 0; i < 3; ++i) {
      bf16x8 f = trfrag((const LDSP char*)lw + ra[i], 1280);
      asm volatile("" : "+v"(f));
      if (i * 64 + lane < 160) st16(wsr, dst[i], __builtin_bit_cast(u32x4, f));
    }
    WAITVM(0);
  } else if (wid < 7) {
    u32x4 xv[5];
    const int xtok = tg * 128 + hg * 8 + (lane >> 3), p0 = (wid - 5) * 5;
#pragma unroll
    for (int i = 0; i < 5; ++i) xv[i] = __builtin_nontemporal_load((const u32x4*)(x + xtok * DM + (p0 + i) * 64 + (lane & 7) * 8));
    epv = 0;
    BARRIER();
#pragma unroll
    for (int i = 0; i < 5; ++i) st16(wsr, WS_XI + (xtok >> 5) * 40960 + ((p0 + i) * 4 + ((lane & 7) >> 1)) * 1024 + ((lane & 1) * 32 + (xtok & 31)) * 16, xv[i]);
    WAITVM(0);
  } else {
    const int t16 = tg * 8 + (hg >> 1), isv = hg & 1;
    const unsigned lds0 = (unsigned)(uintptr_t)lds;
    const char* wkv = (const char*)(isv ? wv : wk);
    const int tk = lane & 15, G = lane >> 4;
    const unsigned lx = lds0 - L_KVR + 3 * STG_B;
    const unsigned xo0 = (lane >> 3) * 1280 + (((lane & 7) ^ (lane >> 4)) << 4), xo1 = xo0 + 8 * 1280 - 1024 + ((xo0 & 64) ? -64 : 64);
    const char* xr = (const char*)x + t16 * 16 * 1280;
    BARRIER();
#pragma unroll
    for (int i = 0; i < 5; ++i) stage_w(wkv + i * 5120, lane * 16, lds0 + i * 5120);
    __builtin_amdgcn_s_sleep(20);
#pragma unroll
    for (int kb = 0; kb < 10; ++kb) { glds<0>(xr + kb * 128, xo0, lx + kb * 2048); glds<1024>(xr + kb * 128, xo1, lx + kb * 2048); }
    glds<0>(gw, lane * 16, lds0 + 25600); glds<0>(gw, min(1024 + lane * 16, 1264), lds0 + 26624);
    const LDSP char* xa = (const LDSP char*)lds - L_KVR + 3 * STG_B + tk * 128 + ((G ^ (tk >> 1)) << 4);
    const LDSP char* xb = (const LDSP char*)lds - L_KVR + 3 * STG_B + tk * 128 + ((G ^ (tk >> 1) ^ 4) << 4);
    auto xf = [&](int ks) { return *(const LDSP bf16x8*)(((ks & 1) ? xb : xa) + (ks >> 1) * 2048); };
    auto gf = [&](int ks) { return *(const LDSP bf16x8*)((const LDSP char*)lds + 25600 + ks * 64 + G * 16); };
    epv = 0;
    WAITVM(0);
    const int q = (lane & 15) >> 2, p = lane & 3;
    const int cg0 = isv ? p : (p < 2 ? p : 4), cg1 = isv ? 4 : (p == 0 ? 2 : 3);
    const LDSP char* b0 = (const LDSP char*)lds + (8 * G + q) * 40 + cg0 * 8;
    const LDSP char* b1 = (const LDSP char*)lds + (8 * G + q) * 40 + cg1 * 8;
    f32x4 a0 = {}, a1 = {}, ag = {};
    const unsigned tb = WS_KV + (t16 >> 3) * CH_B + ((t16 >> 1) & 3) * TILE_B;
    auto run = [&](auto vtag) {
      constexpr bool V = decltype(vtag)::value;
      bf16x8 fw0[2][4], fw1[2][4], fx[2][4], fg[2][4];
      auto rd = [&](int b, int k0) {
#pragma unroll
        for (int i = 0; i < 4; ++i) { fx[b][i] = xf(k0 + i); fw0[b][i] = trfrag(b0 + (k0 + i) * 1280, 160); fw1[b][i] = trfrag(b1 + (k0 + i) * 1280, 160); if (!V) fg[b][i] = gf(k0 + i); } };
      rd(0, 0);
#pragma unroll
      for (int bt = 0; bt < 5; ++bt) {
        SBAR();
        if (bt < 4) rd((bt + 1) & 1, bt * 4 + 4);
        SBAR();
#pragma unroll
        for (int i = 0; i < 4; ++i) {
          const bf16x8 xv = fx[bt & 1][i], u0 = fw0[bt & 1][i], u1 = fw1[bt & 1][i];
          if (!V) { a0 = MFMA16(u0, xv, a0); a1 = MFMA16(u1, xv, a1); ag = MFMA16(fg[bt & 1][i], xv, ag); }
          else { a0 = MFMA16(xv, u0, a0); a1 = MFMA16(xv, u1, a1); }
        }
      } };
    if (!isv) {
      run(BoolTag<false>{});
      const int key = (t16 & 1) * 16 + tk;
      a0 *= QSCALE; a1 *= QSCALE;
      const unsigned w0 = cvtpk(a0[0], a0[1]), w1 = cvtpk(a0[2], a0[3]);
      if (G < 2) st16(wsr, tb + (G * 32 + key) * 16, (u32x4){w0, w1, cvtpk(a1[0], a1[1]), cvtpk(a1[2], a1[3])});
      else if (G == 2) st8(wsr, WS_KV + (t16 >> 3) * CH_B + OFF_K1 + ((t16 >> 2) & 1) * 512 + key * 16 + ((t16 >> 1) & 1) * 8, (u32x2){w0, w1});
      if (G == 0) st4(wsr, WS_G + (t16 * 16 + tk) * 4, __float_as_uint(1.f / (1.f + __expf(-ag[0]))));
    } else {
      run(BoolTag<true>{});
      const unsigned vb = tb + OFF_V + (t16 & 1) * VB + (G >> 1) * 352 + (G & 1) * 8;
      st8(wsr, vb + tk * 16, (u32x2){cvtpk(a0[0], a0[1]), cvtpk(a0[2], a0[3])});
      const unsigned c = tk == 4 ? 0x3F803F80u : 0u;
      if (tk < 6) st8(wsr, vb + (16 + tk) * 16, tk < 4 ? (u32x2){cvtpk(a1[0], a1[1]), cvtpk(a1[2], a1[3])} : (u32x2){c, c});
    }
  }
}

__device__ __noinline__ f32x16 attn_safe(const char* kv, bf16x8 q0, s16x4 q1, int lane) {
  const int r32 = lane & 31, h = lane >> 5, vrow = r32 < 21 ? r32 : 21;
  const unsigned long long kp = (unsigned long long)kv;
  const rsrc_t r = mkrsrc((const void*)(((unsigned long long)__builtin_amdgcn_readfirstlane((unsigned)(kp >> 32)) << 32) | (unsigned)__builtin_amdgcn_readfirstlane((unsigned)kp)), 16 * CH_B);
  f32x16 o = {}; float m = -3.0e38f; const f32x16 z = {};
  for (int t = 0; t < 64; ++t) {
    const unsigned tb = (t >> 2) * CH_B + (t & 3) * TILE_B;
    const bf16x8 k0 = __builtin_bit_cast(bf16x8, ld16(r, lane * 16, tb)); const s16x4 k1 = __builtin_bit_cast(s16x4, ld8(r, (t >> 2) * CH_B + OFF_K1 + ((t >> 1) & 1) * 512 + r32 * 16 + (t & 1) * 8));
    const bf16x8 v0 = __builtin_bit_cast(bf16x8, ld16(r, OFF_V + (h * 22 + vrow) * 16, tb)), v1 = __builtin_bit_cast(bf16x8, ld16(r, OFF_V + VB + (h * 22 + vrow) * 16, tb));
    f32x16 s = MFMA32K8(k1, q1, z); s = MFMA32(k0, q0, s);
    float mt = s[0];
#pragma unroll
    for (int i = 1; i < 16; ++i) mt = fmaxf(mt, s[i]);
    mt = fmaxf(mt, xhalf(mt));
    const float mn = fmaxf(m, mt), al = __builtin_amdgcn_exp2f(m - mn); m = mn;
#pragma unroll
    for (int i = 0; i < 16; ++i) { o[i] *= al; s[i] = __builtin_amdgcn_exp2f(s[i] - mn); }
    o = SMFMAC(v0, __builtin_shufflevector(s, s, 0, 1, 2, 3, 4, 5, 6, 7), o);
    o = SMFMAC(v1, __builtin_shufflevector(s, s, 8, 9, 10, 11, 12, 13, 14, 15), o);
  }
  return o;
}


DI void band_mask(f32x16& ma, f32x4& mb, int tg, int wid, int lane) {
  const int r = lane & 31, h = lane >> 5, lo = (tg == 0 && wid < 4) ? 8 : 0;
#pragma unroll
  for (int i = 0; i < 16; ++i) { const int a = (i & 3) + 8 * (i >> 2) + 4 * h; ma[i] = ((unsigned)(a - r - 1) < 8u && a >= lo) ? 0.f : -1.0e30f; }
#pragma unroll
  for (int i = 0; i < 4; ++i) mb[i] = (i + 4 * h + 24 <= r) ? 0.f : -1.0e30f;
  asm volatile("" : "+v"(ma), "+v"(mb));
}
DI void local_attn(const LDSP char* L, rsrc_t oi, const f32x16& ma, const f32x4& mb, int tg, int hg, int wid, int lane) {
  const int r32 = lane & 31, h = lane >> 5, hd = wid & 1, par = (wid >> 1) & 1, gq = wid >> 2, tl = 2 * (32 * gq + r32) + par, tok = tg * 128 + tl;
  const LDSP char* base = L + L_BS + (par * 72 + 32 * gq) * BS_ROW + hd * 144;
  const LDSP char* rk = base + r32 * BS_ROW;
  const LDSP char* rb = base + (32 + (r32 & 7)) * BS_ROW;
  const LDSP char* rv = base + (8 * (lane >> 5) + ((lane & 15) >> 2)) * BS_ROW + 96 + ((lane >> 4) & 1) * 32 + (lane & 3) * 8;
  const bf16x8 q0 = *(const LDSP bf16x8*)(rk + 8 * BS_ROW + h * 16), ka0 = *(const LDSP bf16x8*)(rk + 48 + h * 16), kb0 = *(const LDSP bf16x8*)(rb + 48 + h * 16);
  u32x2 q1 = *(const LDSP u32x2*)(rk + 8 * BS_ROW + 32);
  const s16x4 ka1 = *(const LDSP s16x4*)(rk + 80), kb1 = *(const LDSP s16x4*)(rb + 80);
  const bf16x8 v0 = trfrag(rv, 4 * BS_ROW), v1 = trfrag(rv + 16 * BS_ROW, 4 * BS_ROW), v2 = trfrag(rv + (32 - 8 * h) * BS_ROW, 4 * BS_ROW);
  const float gate = *(const LDSP float*)(L + L_G + (tg == 15 ? 512 : 0) + tl * 4);
  if (h) q1 = (u32x2){0u, 0u};
  f32x16 cb = {};
#pragma unroll
  for (int i = 0; i < 4; ++i) cb[i] = mb[i];
  f32x16 sa = MFMA32K8(ka1, __builtin_bit_cast(s16x4, q1), ma), sb = MFMA32K8(kb1, __builtin_bit_cast(s16x4, q1), cb);
  sa = MFMA32(ka0, q0, sa); sb = MFMA32(kb0, q0, sb);
  float m = fmaxf(fmaxf(sb[0], sb[1]), fmaxf(sb[2], sb[3]));
#pragma unroll
  for (int i = 0; i < 16; ++i) m = fmaxf(m, sa[i]);
  { auto r = __builtin_amdgcn_permlane32_swap(__float_as_uint(m), __float_as_uint(m), false, false); m = fmaxf(__uint_as_float(r[0]), __uint_as_float(r[1])); }
  const float nm = -m * QSCALE;
#pragma unroll
  for (int i = 0; i < 16; ++i) sa[i] = __builtin_amdgcn_exp2f(__builtin_fmaf(sa[i], QSCALE, nm));
  f32x8 pb = {};
#pragma unroll
  for (int i = 0; i < 4; ++i) pb[i] = __builtin_amdgcn_exp2f(__builtin_fmaf(sb[i], QSCALE, nm));
  f32x16 o = {};
  o = SMFMAC(v0, __builtin_shufflevector(sa, sa, 0, 1, 2, 3, 4, 5, 6, 7), o);
  o = SMFMAC(v1, __builtin_shufflevector(sa, sa, 8, 9, 10, 11, 12, 13, 14, 15), o);
  o = SMFMAC(v2, pb, o);
  const float xl = xhalf(o[8]), sc = (1.f - gate) * __builtin_amdgcn_rcpf(h ? o[8] : xl);
  const unsigned op = (tok >> 4) * 40960 + 20480 + hg * 1280 + (tok & 15) * 16;
  st16(oi, op + (hd * 2 + h) * 256, (u32x4){cvtpk(o[0] * sc, o[1] * sc), cvtpk(o[2] * sc, o[3] * sc), cvtpk(o[4] * sc, o[5] * sc), cvtpk(o[6] * sc, o[7] * sc)});
  if (!h) st8(oi, op + 1024 + hd * 8, (u32x2){cvtpk(o[8] * sc, o[9] * sc), cvtpk(o[10] * sc, o[11] * sc)});
}

__global__ __launch_bounds__(512, 2) void k_main(const u16* __restrict__ x, const u16* __restrict__ wq, const u16* __restrict__ wk, const u16* __restrict__ wv,
                                              const u16* __restrict__ wqkv, const u16* __restrict__ gw, const u16* __restrict__ woA, const u16* __restrict__ woB, char* __restrict__ ws, u16* __restrict__ out) {
  extern __shared__ __attribute__((aligned(16))) char lds[];
  const int lane = threadIdx.x & 63, wid = __builtin_amdgcn_readfirstlane(threadIdx.x >> 6);
  const int r32 = lane & 31, h = lane >> 5;
  int tg, hg; block_map(blockIdx.x, tg, hg);
  const unsigned lds0 = (unsigned)(uintptr_t)lds;
  const LDSP char* L = (const LDSP char*)lds;
  LDSP char* LW = (LDSP char*)lds;
  asm volatile("" ::"s"(x), "s"(wq), "s"(wk), "s"(wv), "s"(wqkv), "s"(gw), "s"(woA), "s"(woB), "s"(ws), "s"(out));
  const rsrc_t oi = mkrsrc(ws + WS_OI, 5242880);
  const int cgp = hg & 7, tq = tg * 8 + (hg >> 3) * 4;
  const rsrc_t wsr = mkrsrc(ws, (unsigned)WS_OI);
  unsigned epv;
  volatile LDSP unsigned* arrive = (volatile LDSP unsigned*)(LW + L_G + 1024);
  phase0(x, wq, wk, wv, wqkv, gw, wsr, lds + L_KVR, tg, hg, wid, lane, epv, arrive);
  unsigned ep = __builtin_amdgcn_readfirstlane(epv) + 1u;
  if (wid < 7) arrive[wid] = 1u;
  if (wid >= 4) {
    f32x16 bma, bma2; f32x4 bmb;
    auto masks = [&] { band_mask(bma2, bmb, tg, wid - 4, lane); band_mask(bma, bmb, tg, wid, lane); };
    if (wid == 4) {
      arrive[8] = ep; arrive[10] = 1u;
      for (unsigned sp = 0; sp < SPIN_CAP * 16 && __builtin_amdgcn_ballot_w64(arrive[min(lane, 6)] == 0u) != 0; ++sp) {}
      if (lane == 0) st4(wsr, WS_F1 + (tg * 16 + hg) * 128, ep);
      masks();
      const int f0 = (lane >> 2) * 16 + (hg & 12) + (lane & 3), f1 = lane < 16 ? tg * 16 + lane : max(tg - 1, 0) * 16 + 14 + (lane & 1);
      unsigned a0 = ld4(wsr, WS_F1 + f0 * 128), b0 = ld4(wsr, WS_F1 + f1 * 128);
      for (unsigned sp = 0; sp < SPIN_CAP; ++sp) {
        asm volatile("s_sleep 2" ::: "memory");
        const unsigned a1 = ld4(wsr, WS_F1 + f0 * 128), b1 = ld4(wsr, WS_F1 + f1 * 128);
        SBAR();
        if (__builtin_amdgcn_ballot_w64(a0 != ep || b0 != ep) == 0) break;
        asm volatile("s_sleep 2" ::: "memory");
        a0 = ld4(wsr, WS_F1 + f0 * 128); b0 = ld4(wsr, WS_F1 + f1 * 128);
        SBAR();
        if (__builtin_amdgcn_ballot_w64(a1 != ep || b1 != ep) == 0) break;
      }
      arrive[9] = 1u;
    } else if (wid == 7) {
      masks();
      for (unsigned sp = 0; sp < SPIN_CAP * 64 && arrive[10] == 0u; ++sp) __builtin_amdgcn_s_sleep(1);
      ep = __builtin_amdgcn_readfirstlane(arrive[8]);
    } else {
      masks();
      for (unsigned sp = 0; sp < SPIN_CAP * 64 && arrive[9] == 0u; ++sp) __builtin_amdgcn_s_sleep(1);
      ep = __builtin_amdgcn_readfirstlane(arrive[8]);
    }
    const int lw = wid - 4;
    const unsigned lo = lane * 16;
    auto kv4 = [&](int c, int slot) { const char* s = ws + WS_KV + c * CH_B + lw * 3072; const unsigned m = lds0 + L_KVR + slot * CH_B + lw * 3072;
      glds<0>(s, lo, m); glds<1024>(s, lo, m); if (lw < 3) glds<2048>(s, lo, m); };
    auto kv2 = [&](int c, int slot) { const char* s = ws + WS_KV + c * CH_B + lw * 5120; const unsigned m = lds0 + L_KVR + slot * CH_B + lw * 5120;
      stage_w(s, lo, m); if (lw == 1) glds<0>(s + 5120, lo, m + 5120); };
    const int tk = lane & 15, G = lane >> 4;
    u32x4 fk;
    {
      const char* wsrc = ws + WS_WP + hg * WP_HG + lw * 5120;
      const unsigned mw = lds0 + lw * 5120;
      if (lw < 3) {
        const char* w3 = ws + WS_WP + hg * WP_HG + 15360 + lw * 2048; const unsigned m3 = lds0 + 15360 + lw * 2048;
        auto extra = [&](int o) { glds<0>(w3 + o, lo, m3 + o); if (lw < 2) glds<1024>(w3 + o, lo, m3 + o); };
        stage_w(wsrc, lo, mw); extra(0);
        const char* hp = ws + WS_XI + max(tg * 4 - 1, 0) * 40960; const unsigned ho = (G >> 1) * 1024 + ((G & 1) * 32 + 16 + tk) * 16;
        if (lw == 2) { glds<0>(hp, ho, lds0 + L_HX); }
        if (lw == 1) { glds<0>(hp + 2048, ho, lds0 + L_HX + 1024); }
        stage_w(wsrc + STG_B, lo, mw + STG_B); extra(STG_B);
      }
      __builtin_amdgcn_s_setprio(3);
      if (lw < 3) WAITVM(6);
      BARRIER();
      { const char* hp = ws + WS_XI + max(tg * 4 - 1, 0) * 40960 + 4096; const unsigned ho = (G >> 1) * 1024 + ((G & 1) * 32 + 16 + tk) * 16;
#pragma unroll
        for (int i = 0; i < 5; ++i) if (lw * 5 + i < 18) glds<0>(hp + (lw * 5 + i) * 2048, ho, lds0 + L_HX + 2048 + (lw * 5 + i) * 1024);
        stage_w(wsrc + 2 * STG_B, lo, mw + 2 * STG_B); stage_w(wsrc + 3 * STG_B, lo, mw + 3 * STG_B); }
      auto st = [&](int s) { stage_w(wsrc + s * STG_B, lo, mw + (s % 5) * STG_B); };
      WAITVM(10); BARRIER();
      if (lw == 3 && lane == 0) st4(wsr, WS_FL + 2048 + blockIdx.x * 4, ep);
      st(4);
      WAITVM(10); BARRIER(); st(5);
      WAITVM(10); BARRIER(); st(6);
      WAITVM(10); BARRIER();
      asm volatile("global_load_dwordx4 %0, %1, %2 sc1" : "=v"(fk) : "v"(lo), "s"(ws + WS_FL + 2048) : "memory");
      st(7);
      WAITVM(10); BARRIER(); st(8);
      WAITVM(10); BARRIER(); st(9);
      WAITVM(10); BARRIER();
      asm volatile("" : "+v"(fk));
      for (unsigned sp = 0; sp < SPIN_CAP && __builtin_amdgcn_ballot_w64(fk[0] != ep || fk[1] != ep || fk[2] != ep || fk[3] != ep) != 0; ++sp)
        asm volatile("s_sleep 1\n\tglobal_load_dwordx4 %0, %1, %2 sc1\n\ts_waitcnt vmcnt(0)" : "=&v"(fk) : "v"(lo), "s"(ws + WS_FL + 2048) : "memory");
      if (blockIdx.x == 0 && wid == 4 && lane == 0) st4(wsr, WS_FL + 3072, ep);
      if (lw == 3) glds<0>(ws + WS_G + min(tg * 512, 7168), lo, lds0 + L_G);
      kv4(0, 0); kv4(1, 1); kv4(2, 2);
      if (lw < 3) WAITVM(14); else WAITVM(11);
      BARRIER();
#pragma unroll
      for (int i = 0; i < 3; ++i) {
        const int e = min(i * 64 + lane, 159), tt2 = e >= 80; st16(oi, (tg * 8 + lw * 2 + tt2) * 40960 + hg * 1280 + (e - tt2 * 80) * 16, (u32x4)(0xFFFFFFFFu)); }
      if (lw < 3) WAITVM(12); else WAITVM(9);
      BARRIER();
    }
    WAITVM(3);
    BARRIER();
    local_attn(L, oi, bma, bmb, tg, hg, wid, lane);
    local_attn(L, oi, bma2, bmb, tg, hg, wid - 4, lane);
    const char* wop = ws + WS_WO + cgp * 204800;
    auto period = [&](int c, int g) {
      BARRIER();
      if (lw < 2) { if (c < 15) kv2(c + 1, (c + 1) % 3); }
      else if (g >= 0) { const int o = (lw - 2) * 51200 + g * 10240; stage_w(wop + o, lo, lds0 + o); stage_w(wop + o + 5120, lo, lds0 + o + 5120); } };
#define KVW(n) do { if (lw < 2) WAITVM(n); } while (0)
    bf16x8 wb[25]; u32x4 of[20];
    const unsigned wbo = WS_WO + cgp * 204800 + 102400 + lw * 25600, ob = tq * 40960 + 20480 + lw * 5120;
#define WBL(a) _Pragma("unroll") for (int i = a; i < a + 5; ++i) wb[i] = __builtin_bit_cast(bf16x8, ld16(wsr, lo, wbo + i * 1024))
#define OBL(a) _Pragma("unroll") for (int i = a; i < a + 5; ++i) of[i] = ld16(oi, lo + (i % 5) * 1024, ob + (i / 5) * 40960)
    WAITVM(0);
    period(2, 0);
    if (wid == 4 && lane == 0) st4(wsr, WS_FL + 1024 + (tg * 16 + hg) * 4, ep);
    KVW(0); period(3, 1); KVW(0); period(4, 2); KVW(0); period(5, 3); KVW(0); period(6, 4);
    unsigned fv = ld4(wsr, WS_FL + 1024 + tg * 64 + (lane & 15) * 4);
    KVW(0); period(7, -1);
    if (lw == 0) for (unsigned sp = 0; sp < SPIN_CAP && __builtin_amdgcn_ballot_w64(fv != ep) != 0; ++sp) { asm volatile("s_sleep 2" ::: "memory"); fv = ld4(wsr, WS_FL + 1024 + tg * 64 + (lane & 15) * 4); }
    WBL(0);
    KVW(5); period(8, -1); WBL(5); WBL(10);
    KVW(10); period(9, -1); WBL(15); WBL(20);
    KVW(10); period(10, -1); OBL(0);
    KVW(5); period(11, -1); OBL(5);
    KVW(5); period(12, -1); OBL(10);
    KVW(5); period(13, -1); OBL(15);
    KVW(5); period(14, -1); KVW(0); period(15, -1);
    WAITVM(0);
    BARRIER();
    BARRIER();
    __builtin_amdgcn_s_sleep(5);
    LDSP f32x4* red = (LDSP f32x4*)(LW + L_HX) + lane;
    f32x4 keep[5];
#pragma unroll
    for (int hf = 0; hf < 2; ++hf) {
      f32x4 acc[10] = {};
#pragma unroll
      for (int ks = 0; ks < 5; ++ks)
#pragma unroll
        for (int i = 0; i < 10; ++i) acc[i] = MFMA16(wb[ks * 5 + i % 5], __builtin_bit_cast(bf16x8, of[hf * 10 + (i / 5) * 5 + ks]), acc[i]);
#pragma unroll
      for (int q = 0; q < 2; ++q) {
        const int tt = hf * 2 + q;
        if (tt != lw) {
#pragma unroll
          for (int ct = 0; ct < 5; ++ct) red[(lw * 15 + (tt - (tt > lw)) * 5 + ct) * 64] = acc[q * 5 + ct];
        } else {
#pragma unroll
          for (int ct = 0; ct < 5; ++ct) keep[ct] = acc[q * 5 + ct];
        }
      }
    }
    BARRIER();
    { int sl[3];
#pragma unroll
      for (int d = 0; d < 3; ++d) { const int j = (lw + 1 + d) & 3; sl[d] = j * 15 + (lw - (lw > j)) * 5; }
#pragma unroll
      for (int ct = 0; ct < 5; ++ct) red[(sl[0] + ct) * 64] = (keep[ct] + red[(sl[0] + ct) * 64]) + (red[(sl[1] + ct) * 64] + red[(sl[2] + ct) * 64]); }
    BARRIER();
    return;
  }
  if (wid < 2 || (wid == 2 && blockIdx.x < 128)) repack_wo(woA, woB, wsr, lds + 4 * STG_B + wid * 3072, wid * 256 + blockIdx.x, lane);
  const int tok = tg * 128 + wid * 32 + r32;
  f32x16 acc[5] = {}; f32x4 ha = {}, hb = {};
  {
    const int tk = lane & 15, G = lane >> 4, hw = lds0 + (G >> 1) * 5120 + ((G & 1) * 32 + tk) * 16;
    unsigned wa = lds0 + lane * 16, xo = lane * 16, hxa = lds0 + L_HX + lane * 16, hw0 = hw + (3 + (wid >> 1)) * 1024 + (wid & 1) * 256, hw1 = hw + 2304;
    const char* xs = ws + WS_XI + (tg * 4 + wid) * 40960;
    asm volatile("" : "+{v[8:23]}"(acc[0]), "+{v[24:39]}"(acc[1]), "+{v[40:55]}"(acc[2]), "+{v[56:71]}"(acc[3]), "+{v[72:87]}"(acc[4]), "+{v[174:177]}"(ha), "+{v[178:181]}"(hb),
                 "+{v168}"(wa), "+{v169}"(xo), "+{v170}"(hxa), "+{v171}"(hw0), "+{v172}"(hw1));
    for (unsigned sp = 0; sp < SPIN_CAP * 64 && arrive[9] == 0u; ++sp) __builtin_amdgcn_s_sleep(1);
    asm volatile(
  "global_load_dwordx4 v[88:91], v169, s[24:25] offset:0 sc1\n"
  "global_load_dwordx4 v[92:95], v169, s[24:25] offset:1024 sc1\n"
  "global_load_dwordx4 v[96:99], v169, s[24:25] offset:2048 sc1\n"
  "global_load_dwordx4 v[100:103], v169, s[24:25] offset:3072 sc1\n"
  "v_add_u32 v169, 0x1000, v169\n"
  "global_load_dwordx4 v[104:107], v169, s[24:25] offset:0 sc1\n"
  "global_load_dwordx4 v[108:111], v169, s[24:25] offset:1024 sc1\n"
  "global_load_dwordx4 v[112:115], v169, s[24:25] offset:2048 sc1\n"
  "global_load_dwordx4 v[116:119], v169, s[24:25] offset:3072 sc1\n"
  "v_add_u32 v169, 0x1000, v169\n"
  "global_load_dwordx4 v[120:123], v169, s[24:25] offset:0 sc1\n"
  "s_mov_b32 s20, 0\n"
  "s_cmp_eq_u32 s26, 0\n"
  "s_cselect_b64 vcc, 1, 0\n"
  "s_barrier\n"
  "ds_read_b128 v[128:131], v168 offset:0\n"
  "ds_read_b128 v[132:135], v168 offset:1024\n"
  "ds_read_b128 v[136:139], v168 offset:2048\n"
  "ds_read_b128 v[140:143], v168 offset:3072\n"
  "ds_read_b128 v[144:147], v168 offset:4096\n"
  "LG%=:\n"
  "s_waitcnt vmcnt(8)\n"
  "s_waitcnt lgkmcnt(4)\n"
  "v_mfma_f32_32x32x16_bf16 v[8:23], v[128:131], v[88:91], v[8:23]\n"
  "ds_read_b128 v[148:151], v168 offset:5120\n"
  "s_waitcnt lgkmcnt(4)\n"
  "v_mfma_f32_32x32x16_bf16 v[24:39], v[132:135], v[88:91], v[24:39]\n"
  "ds_read_b128 v[152:155], v168 offset:6144\n"
  "s_waitcnt lgkmcnt(4)\n"
  "v_mfma_f32_32x32x16_bf16 v[40:55], v[136:139], v[88:91], v[40:55]\n"
  "ds_read_b128 v[156:159], v168 offset:7168\n"
  "s_waitcnt lgkmcnt(4)\n"
  "v_mfma_f32_32x32x16_bf16 v[56:71], v[140:143], v[88:91], v[56:71]\n"
  "ds_read_b128 v[160:163], v168 offset:8192\n"
  "s_waitcnt lgkmcnt(4)\n"
  "v_mfma_f32_32x32x16_bf16 v[72:87], v[144:147], v[88:91], v[72:87]\n"
  "ds_read_b128 v[164:167], v168 offset:9216\n"
  "s_waitcnt vmcnt(7)\n"
  "s_waitcnt lgkmcnt(4)\n"
  "v_mfma_f32_32x32x16_bf16 v[8:23], v[148:151], v[92:95], v[8:23]\n"
  "ds_read_b128 v[128:131], v168 offset:10240\n"
  "s_waitcnt lgkmcnt(4)\n"
  "v_mfma_f32_32x32x16_bf16 v[24:39], v[152:155], v[92:95], v[24:39]\n"
  "ds_read_b128 v[132:135], v168 offset:11264\n"
  "s_waitcnt lgkmcnt(4)\n"
  "v_mfma_f32_32x32x16_bf16 v[40:55], v[156:159], v[92:95], v[40:55]\n"
  "ds_read_b128 v[136:139], v168 offset:12288\n"
  "s_waitcnt lgkmcnt(4)\n"
  "v_mfma_f32_32x32x16_bf16 v[56:71], v[160:163], v[92:95], v[56:71]\n"
  "ds_read_b128 v[140:143], v168 offset:13312\n"
  "s_waitcnt lgkmcnt(4)\n"
  "v_mfma_f32_32x32x16_bf16 v[72:87], v[164:167], v[92:95], v[72:87]\n"
  "ds_read_b128 v[144:147], v168 offset:14336\n"
  "s_waitcnt vmcnt(6)\n"
  "s_waitcnt lgkmcnt(4)\n"
  "v_mfma_f32_32x32x16_bf16 v[8:23], v[128:131], v[96:99], v[8:23]\n"
  "ds_read_b128 v[148:151], v168 offset:15360\n"
  "ds_read_b128 v[152:155], v168 offset:16384\n"
  "global_load_dwordx4 v[124:127], v169, s[24:25] offset:1024 sc1\n"
  "s_waitcnt lgkmcnt(5)\n"
  "v_mfma_f32_32x32x16_bf16 v[24:39], v[132:135], v[96:99], v[24:39]\n"
  "ds_read_b128 v[156:159], v168 offset:17408\n"
  "ds_read_b128 v[160:163], v168 offset:18432\n"
  "global_load_dwordx4 v[88:91], v169, s[24:25] offset:2048 sc1\n"
  "s_waitcnt lgkmcnt(6)\n"
  "v_mfma_f32_32x32x16_bf16 v[40:55], v[136:139], v[96:99], v[40:55]\n"
  "ds_read_b128 v[164:167], v168 offset:19456\n"
  "global_load_dwordx4 v[92:95], v169, s[24:25] offset:3072 sc1\n"
  "v_add_u32 v169, 0x1000, v169\n"
  "s_cmp_eq_u32 s20, 1\n"
  "ds_read_b128 v[182:185], v170 offset:0\n"
  "s_waitcnt lgkmcnt(7)\n"
  "v_mfma_f32_32x32x16_bf16 v[56:71], v[140:143], v[96:99], v[56:71]\n"
  "ds_read_b128 v[186:189], v170 offset:1024\n"
  "ds_read_b128 v[190:193], v171\n"
  "ds_read_b128 v[194:197], v171 offset:10240\n"
  "s_waitcnt lgkmcnt(9)\n"
  "v_mfma_f32_32x32x16_bf16 v[72:87], v[144:147], v[96:99], v[72:87]\n"
  "s_cbranch_vccz LR2%=\n"
  "ds_read_b128 v[198:201], v172\n"
  "ds_read_b128 v[202:205], v172 offset:10240\n"
  "LR2%=:\n"
  "global_load_dwordx4 v[96:99], v169, s[24:25] offset:0 sc1\n"
  "s_waitcnt vmcnt(9)\n"
  "s_waitcnt lgkmcnt(4)\n"
  "v_mfma_f32_32x32x16_bf16 v[8:23], v[148:151], v[100:103], v[8:23]\n"
  "s_waitcnt lgkmcnt(0)\n"
  "s_barrier\n"
  "v_add_u32 v168, 20480, v168\n"
  "ds_read_b128 v[128:131], v168 offset:0\n"
  "ds_read_b128 v[132:135], v168 offset:1024\n"
  "v_mfma_f32_32x32x16_bf16 v[24:39], v[152:155], v[100:103], v[24:39]\n"
  "ds_read_b128 v[136:139], v168 offset:2048\n"
  "v_add_u32 v171, 20480, v171\n"
  "v_mfma_f32_32x32x16_bf16 v[40:55], v[156:159], v[100:103], v[40:55]\n"
  "ds_read_b128 v[140:143], v168 offset:3072\n"
  "v_add_u32 v172, 20480, v172\n"
  "v_mfma_f32_32x32x16_bf16 v[56:71], v[160:163], v[100:103], v[56:71]\n"
  "ds_read_b128 v[144:147], v168 offset:4096\n"
  "v_mfma_f32_32x32x16_bf16 v[72:87], v[164:167], v[100:103], v[72:87]\n"
  "v_mfma_f32_16x16x32_bf16 v[174:177], v[190:193], v[182:185], v[174:177]\n"
  "v_mfma_f32_16x16x32_bf16 v[174:177], v[194:197], v[186:189], v[174:177]\n"
  "s_cbranch_vccz LH3%=\n"
  "v_mfma_f32_16x16x32_bf16 v[178:181], v[198:201], v[182:185], v[178:181]\n"
  "v_mfma_f32_16x16x32_bf16 v[178:181], v[202:205], v[186:189], v[178:181]\n"
  "LH3%=:\n"
  "s_waitcnt vmcnt(8)\n"
  "s_waitcnt lgkmcnt(4)\n"
  "v_mfma_f32_32x32x16_bf16 v[8:23], v[128:131], v[104:107], v[8:23]\n"
  "ds_read_b128 v[148:151], v168 offset:5120\n"
  "s_waitcnt lgkmcnt(4)\n"
  "v_mfma_f32_32x32x16_bf16 v[24:39], v[132:135], v[104:107], v[24:39]\n"
  "ds_read_b128 v[152:155], v168 offset:6144\n"
  "s_waitcnt lgkmcnt(4)\n"
  "v_mfma_f32_32x32x16_bf16 v[40:55], v[136:139], v[104:107], v[40:55]\n"
  "ds_read_b128 v[156:159], v168 offset:7168\n"
  "s_waitcnt lgkmcnt(4)\n"
  "v_mfma_f32_32x32x16_bf16 v[56:71], v[140:143], v[104:107], v[56:71]\n"
  "ds_read_b128 v[160:163], v168 offset:8192\n"
  "s_waitcnt lgkmcnt(4)\n"
  "v_mfma_f32_32x32x16_bf16 v[72:87], v[144:147], v[104:107], v[72:87]\n"
  "ds_read_b128 v[164:167], v168 offset:9216\n"
  "s_waitcnt vmcnt(7)\n"
  "s_waitcnt lgkmcnt(4)\n"
  "v_mfma_f32_32x32x16_bf16 v[8:23], v[148:151], v[108:111], v[8:23]\n"
  "ds_read_b128 v[128:131], v168 offset:10240\n"
  "s_waitcnt lgkmcnt(4)\n"
  "v_mfma_f32_32x32x16_bf16 v[24:39], v[152:155], v[108:111], v[24:39]\n"
  "ds_read_b128 v[132:135], v168 offset:11264\n"
  "s_waitcnt lgkmcnt(4)\n"
  "v_mfma_f32_32x32x16_bf16 v[40:55], v[156:159], v[108:111], v[40:55]\n"
  "ds_read_b128 v[136:139], v168 offset:12288\n"
  "s_waitcnt lgkmcnt(4)\n"
  "v_mfma_f32_32x32x16_bf16 v[56:71], v[160:163], v[108:111], v[56:71]\n"
  "ds_read_b128 v[140:143], v168 offset:13312\n"
  "s_waitcnt lgkmcnt(4)\n"
  "v_mfma_f32_32x32x16_bf16 v[72:87], v[164:167], v[108:111], v[72:87]\n"
  "ds_read_b128 v[144:147], v168 offset:14336\n"
  "s_waitcnt vmcnt(6)\n"
  "s_waitcnt lgkmcnt(4)\n"
  "v_mfma_f32_32x32x16_bf16 v[8:23], v[128:131], v[112:115], v[8:23]\n"
  "ds_read_b128 v[148:151], v168 offset:15360\n"
  "ds_read_b128 v[152:155], v168 offset:16384\n"
  "global_load_dwordx4 v[100:103], v169, s[24:25] offset:1024 sc1\n"
  "s_waitcnt lgkmcnt(5)\n"
  "v_mfma_f32_32x32x16_bf16 v[24:39], v[132:135], v[112:115], v[24:39]\n"
  "ds_read_b128 v[156:159], v168 offset:17408\n"
  "ds_read_b128 v[160:163], v168 offset:18432\n"
  "global_load_dwordx4 v[104:107], v169, s[24:25] offset:2048 sc1\n"
  "s_waitcnt lgkmcnt(6)\n"
  "v_mfma_f32_32x32x16_bf16 v[40:55], v[136:139], v[112:115], v[40:55]\n"
  "ds_read_b128 v[164:167], v168 offset:19456\n"
  "global_load_dwordx4 v[108:111], v169, s[24:25] offset:3072 sc1\n"
  "v_add_u32 v169, 0x1000, v169\n"
  "ds_read_b128 v[182:185], v170 offset:2048\n"
  "s_waitcnt lgkmcnt(7)\n"
  "v_mfma_f32_32x32x16_bf16 v[56:71], v[140:143], v[112:115], v[56:71]\n"
  "ds_read_b128 v[186:189], v170 offset:3072\n"
  "ds_read_b128 v[190:193], v171\n"
  "ds_read_b128 v[194:197], v171 offset:10240\n"
  "s_waitcnt lgkmcnt(9)\n"
  "v_mfma_f32_32x32x16_bf16 v[72:87], v[144:147], v[112:115], v[72:87]\n"
  "s_cbranch_vccz LR6%=\n"
  "ds_read_b128 v[198:201], v172\n"
  "ds_read_b128 v[202:205], v172 offset:10240\n"
  "LR6%=:\n"
  "global_load_dwordx4 v[112:115], v169, s[24:25] offset:0 sc1\n"
  "s_waitcnt vmcnt(9)\n"
  "s_waitcnt lgkmcnt(4)\n"
  "v_mfma_f32_32x32x16_bf16 v[8:23], v[148:151], v[116:119], v[8:23]\n"
  "s_waitcnt lgkmcnt(0)\n"
  "s_barrier\n"
  "v_add_u32 v168, 20480, v168\n"
  "ds_read_b128 v[128:131], v168 offset:0\n"
  "ds_read_b128 v[132:135], v168 offset:1024\n"
  "v_mfma_f32_32x32x16_bf16 v[24:39], v[152:155], v[116:119], v[24:39]\n"
  "ds_read_b128 v[136:139], v168 offset:2048\n"
  "v_add_u32 v171, 20480, v171\n"
  "v_mfma_f32_32x32x16_bf16 v[40:55], v[156:159], v[116:119], v[40:55]\n"
  "ds_read_b128 v[140:143], v168 offset:3072\n"
  "v_add_u32 v172, 20480, v172\n"
  "v_mfma_f32_32x32x16_bf16 v[56:71], v[160:163], v[116:119], v[56:71]\n"
  "ds_read_b128 v[144:147], v168 offset:4096\n"
  "v_mfma_f32_32x32x16_bf16 v[72:87], v[164:167], v[116:119], v[72:87]\n"
  "v_mfma_f32_16x16x32_bf16 v[174:177], v[190:193], v[182:185], v[174:177]\n"
  "v_mfma_f32_16x16x32_bf16 v[174:177], v[194:197], v[186:189], v[174:177]\n"
  "s_cbranch_vccz LH7%=\n"
  "v_mfma_f32_16x16x32_bf16 v[178:181], v[198:201], v[182:185], v[178:181]\n"
  "v_mfma_f32_16x16x32_bf16 v[178:181], v[202:205], v[186:189], v[178:181]\n"
  "LH7%=:\n"
  "s_waitcnt vmcnt(8)\n"
  "s_waitcnt lgkmcnt(4)\n"
  "v_mfma_f32_32x32x16_bf16 v[8:23], v[128:131], v[120:123], v[8:23]\n"
  "ds_read_b128 v[148:151], v168 offset:5120\n"
  "s_waitcnt lgkmcnt(4)\n"
  "v_mfma_f32_32x32x16_bf16 v[24:39], v[132:135], v[120:123], v[24:39]\n"
  "ds_read_b128 v[152:155], v168 offset:6144\n"
  "s_waitcnt lgkmcnt(4)\n"
  "v_mfma_f32_32x32x16_bf16 v[40:55], v[136:139], v[120:123], v[40:55]\n"
  "ds_read_b128 v[156:159], v168 offset:7168\n"
  "s_waitcnt lgkmcnt(4)\n"
  "v_mfma_f32_32x32x16_bf16 v[56:71], v[140:143], v[120:123], v[56:71]\n"
  "ds_read_b128 v[160:163], v168 offset:8192\n"
  "s_waitcnt lgkmcnt(4)\n"
  "v_mfma_f32_32x32x16_bf16 v[72:87], v[144:147], v[120:123], v[72:87]\n"
  "ds_read_b128 v[164:167], v168 offset:9216\n"
  "s_waitcnt vmcnt(7)\n"
  "s_waitcnt lgkmcnt(4)\n"
  "v_mfma_f32_32x32x16_bf16 v[8:23], v[148:151], v[124:127], v[8:23]\n"
  "ds_read_b128 v[128:131], v168 offset:10240\n"
  "s_waitcnt lgkmcnt(4)\n"
  "v_mfma_f32_32x32x16_bf16 v[24:39], v[152:155], v[124:127], v[24:39]\n"
  "ds_read_b128 v[132:135], v168 offset:11264\n"
  "s_waitcnt lgkmcnt(4)\n"
  "v_mfma_f32_32x32x16_bf16 v[40:55], v[156:159], v[124:127], v[40:55]\n"
  "ds_read_b128 v[136:139], v168 offset:12288\n"
  "s_waitcnt lgkmcnt(4)\n"
  "v_mfma_f32_32x32x16_bf16 v[56:71], v[160:163], v[124:127], v[56:71]\n"
  "ds_read_b128 v[140:143], v168 offset:13312\n"
  "s_waitcnt lgkmcnt(4)\n"
  "v_mfma_f32_32x32x16_bf16 v[72:87], v[164:167], v[124:127], v[72:87]\n"
  "ds_read_b128 v[144:147], v168 offset:14336\n"
  "s_waitcnt vmcnt(6)\n"
  "s_waitcnt lgkmcnt(4)\n"
  "v_mfma_f32_32x32x16_bf16 v[8:23], v[128:131], v[88:91], v[8:23]\n"
  "ds_read_b128 v[148:151], v168 offset:15360\n"
  "ds_read_b128 v[152:155], v168 offset:16384\n"
  "global_load_dwordx4 v[116:119], v169, s[24:25] offset:1024 sc1\n"
  "s_waitcnt lgkmcnt(5)\n"
  "v_mfma_f32_32x32x16_bf16 v[24:39], v[132:135], v[88:91], v[24:39]\n"
  "ds_read_b128 v[156:159], v168 offset:17408\n"
  "ds_read_b128 v[160:163], v168 offset:18432\n"
  "global_load_dwordx4 v[120:123], v169, s[24:25] offset:2048 sc1\n"
  "s_waitcnt lgkmcnt(6)\n"
  "v_mfma_f32_32x32x16_bf16 v[40:55], v[136:139], v[88:91], v[40:55]\n"
  "ds_read_b128 v[164:167], v168 offset:19456\n"
  "global_load_dwordx4 v[124:127], v169, s[24:25] offset:3072 sc1\n"
  "v_add_u32 v169, 0x1000, v169\n"
  "ds_read_b128 v[182:185], v170 offset:4096\n"
  "s_waitcnt lgkmcnt(7)\n"
  "v_mfma_f32_32x32x16_bf16 v[56:71], v[140:143], v[88:91], v[56:71]\n"
  "ds_read_b128 v[186:189], v170 offset:5120\n"
  "ds_read_b128 v[190:193], v171\n"
  "ds_read_b128 v[194:197], v171 offset:10240\n"
  "s_waitcnt lgkmcnt(9)\n"
  "v_mfma_f32_32x32x16_bf16 v[72:87], v[144:147], v[88:91], v[72:87]\n"
  "s_cbranch_vccz LR10%=\n"
  "ds_read_b128 v[198:201], v172\n"
  "ds_read_b128 v[202:205], v172 offset:10240\n"
  "LR10%=:\n"
  "global_load_dwordx4 v[88:91], v169, s[24:25] offset:0 sc1\n"
  "s_waitcnt vmcnt(9)\n"
  "s_waitcnt lgkmcnt(4)\n"
  "v_mfma_f32_32x32x16_bf16 v[8:23], v[148:151], v[92:95], v[8:23]\n"
  "s_waitcnt lgkmcnt(0)\n"
  "s_barrier\n"
  "v_add_u32 v168, 20480, v168\n"
  "ds_read_b128 v[128:131], v168 offset:0\n"
  "ds_read_b128 v[132:135], v168 offset:1024\n"
  "v_mfma_f32_32x32x16_bf16 v[24:39], v[152:155], v[92:95], v[24:39]\n"
  "ds_read_b128 v[136:139], v168 offset:2048\n"
  "v_add_u32 v171, 20480, v171\n"
  "v_mfma_f32_32x32x16_bf16 v[40:55], v[156:159], v[92:95], v[40:55]\n"
  "ds_read_b128 v[140:143], v168 offset:3072\n"
  "v_add_u32 v172, 20480, v172\n"
  "v_mfma_f32_32x32x16_bf16 v[56:71], v[160:163], v[92:95], v[56:71]\n"
  "ds_read_b128 v[144:147], v168 offset:4096\n"
  "v_mfma_f32_32x32x16_bf16 v[72:87], v[164:167], v[92:95], v[72:87]\n"
  "v_mfma_f32_16x16x32_bf16 v[174:177], v[190:193], v[182:185], v[174:177]\n"
  "v_mfma_f32_16x16x32_bf16 v[174:177], v[194:197], v[186:189], v[174:177]\n"
  "s_cbranch_vccz LH11%=\n"
  "v_mfma_f32_16x16x32_bf16 v[178:181], v[198:201], v[182:185], v[178:181]\n"
  "v_mfma_f32_16x16x32_bf16 v[178:181], v[202:205], v[186:189], v[178:181]\n"
  "LH11%=:\n"
  "s_waitcnt vmcnt(8)\n"
  "s_waitcnt lgkmcnt(4)\n"
  "v_mfma_f32_32x32x16_bf16 v[8:23], v[128:131], v[96:99], v[8:23]\n"
  "ds_read_b128 v[148:151], v168 offset:5120\n"
  "s_waitcnt lgkmcnt(4)\n"
  "v_mfma_f32_32x32x16_bf16 v[24:39], v[132:135], v[96:99], v[24:39]\n"
  "ds_read_b128 v[152:155], v168 offset:6144\n"
  "s_waitcnt lgkmcnt(4)\n"
  "v_mfma_f32_32x32x16_bf16 v[40:55], v[136:139], v[96:99], v[40:55]\n"
  "ds_read_b128 v[156:159], v168 offset:7168\n"
  "s_waitcnt lgkmcnt(4)\n"
  "v_mfma_f32_32x32x16_bf16 v[56:71], v[140:143], v[96:99], v[56:71]\n"
  "ds_read_b128 v[160:163], v168 offset:8192\n"
  "s_waitcnt lgkmcnt(4)\n"
  "v_mfma_f32_32x32x16_bf16 v[72:87], v[144:147], v[96:99], v[72:87]\n"
  "ds_read_b128 v[164:167], v168 offset:9216\n"
  "s_waitcnt vmcnt(7)\n"
  "s_waitcnt lgkmcnt(4)\n"
  "v_mfma_f32_32x32x16_bf16 v[8:23], v[148:151], v[100:103], v[8:23]\n"
  "ds_read_b128 v[128:131], v168 offset:10240\n"
  "s_waitcnt lgkmcnt(4)\n"
  "v_mfma_f32_32x32x16_bf16 v[24:39], v[152:155], v[100:103], v[24:39]\n"
  "ds_read_b128 v[132:135], v168 offset:11264\n"
  "s_waitcnt lgkmcnt(4)\n"
  "v_mfma_f32_32x32x16_bf16 v[40:55], v[156:159], v[100:103], v[40:55]\n"
  "ds_read_b128 v[136:139], v168 offset:12288\n"
  "s_waitcnt lgkmcnt(4)\n"
  "v_mfma_f32_32x32x16_bf16 v[56:71], v[160:163], v[100:103], v[56:71]\n"
  "ds_read_b128 v[140:143], v168 offset:13312\n"
  "s_waitcnt lgkmcnt(4)\n"
  "v_mfma_f32_32x32x16_bf16 v[72:87], v[164:167], v[100:103], v[72:87]\n"
  "ds_read_b128 v[144:147], v168 offset:14336\n"
  "s_waitcnt vmcnt(6)\n"
  "s_waitcnt lgkmcnt(4)\n"
  "v_mfma_f32_32x32x16_bf16 v[8:23], v[128:131], v[104:107], v[8:23]\n"
  "ds_read_b128 v[148:151], v168 offset:15360\n"
  "ds_read_b128 v[152:155], v168 offset:16384\n"
  "s_cbranch_scc1 LS140%=\n"
  "global_load_dwordx4 v[92:95], v169, s[24:25] offset:1024 sc1\n"
  "LS140%=:\n"
  "s_waitcnt lgkmcnt(5)\n"
  "v_mfma_f32_32x32x16_bf16 v[24:39], v[132:135], v[104:107], v[24:39]\n"
  "ds_read_b128 v[156:159], v168 offset:17408\n"
  "ds_read_b128 v[160:163], v168 offset:18432\n"
  "s_cbranch_scc1 LS141%=\n"
  "global_load_dwordx4 v[96:99], v169, s[24:25] offset:2048 sc1\n"
  "LS141%=:\n"
  "s_waitcnt lgkmcnt(6)\n"
  "v_mfma_f32_32x32x16_bf16 v[40:55], v[136:139], v[104:107], v[40:55]\n"
  "ds_read_b128 v[164:167], v168 offset:19456\n"
  "s_cbranch_scc1 LS142%=\n"
  "global_load_dwordx4 v[100:103], v169, s[24:25] offset:3072 sc1\n"
  "v_add_u32 v169, 0x1000, v169\n"
  "LS142%=:\n"
  "ds_read_b128 v[182:185], v170 offset:6144\n"
  "s_waitcnt lgkmcnt(7)\n"
  "v_mfma_f32_32x32x16_bf16 v[56:71], v[140:143], v[104:107], v[56:71]\n"
  "ds_read_b128 v[186:189], v170 offset:7168\n"
  "ds_read_b128 v[190:193], v171\n"
  "ds_read_b128 v[194:197], v171 offset:10240\n"
  "s_waitcnt lgkmcnt(9)\n"
  "v_mfma_f32_32x32x16_bf16 v[72:87], v[144:147], v[104:107], v[72:87]\n"
  "s_cbranch_vccz LR14%=\n"
  "ds_read_b128 v[198:201], v172\n"
  "ds_read_b128 v[202:205], v172 offset:10240\n"
  "LR14%=:\n"
  "s_cbranch_scc1 LS143%=\n"
  "global_load_dwordx4 v[104:107], v169, s[24:25] offset:0 sc1\n"
  "LS143%=:\n"
  "s_waitcnt vmcnt(9)\n"
  "s_cbranch_scc0 LW15%=\n"
  "s_waitcnt vmcnt(5)\n"
  "LW15%=:\n"
  "s_waitcnt lgkmcnt(4)\n"
  "v_mfma_f32_32x32x16_bf16 v[8:23], v[148:151], v[108:111], v[8:23]\n"
  "s_waitcnt lgkmcnt(0)\n"
  "s_barrier\n"
  "v_add_u32 v168, 20480, v168\n"
  "ds_read_b128 v[128:131], v168 offset:0\n"
  "ds_read_b128 v[132:135], v168 offset:1024\n"
  "v_mfma_f32_32x32x16_bf16 v[24:39], v[152:155], v[108:111], v[24:39]\n"
  "ds_read_b128 v[136:139], v168 offset:2048\n"
  "v_add_u32 v171, 20480, v171\n"
  "v_mfma_f32_32x32x16_bf16 v[40:55], v[156:159], v[108:111], v[40:55]\n"
  "ds_read_b128 v[140:143], v168 offset:3072\n"
  "v_add_u32 v172, 20480, v172\n"
  "v_mfma_f32_32x32x16_bf16 v[56:71], v[160:163], v[108:111], v[56:71]\n"
  "ds_read_b128 v[144:147], v168 offset:4096\n"
  "v_mfma_f32_32x32x16_bf16 v[72:87], v[164:167], v[108:111], v[72:87]\n"
  "v_mfma_f32_16x16x32_bf16 v[174:177], v[190:193], v[182:185], v[174:177]\n"
  "v_mfma_f32_16x16x32_bf16 v[174:177], v[194:197], v[186:189], v[174:177]\n"
  "s_cbranch_vccz LH15%=\n"
  "v_mfma_f32_16x16x32_bf16 v[178:181], v[198:201], v[182:185], v[178:181]\n"
  "v_mfma_f32_16x16x32_bf16 v[178:181], v[202:205], v[186:189], v[178:181]\n"
  "LH15%=:\n"
  "s_waitcnt vmcnt(8)\n"
  "s_cbranch_scc0 LW16%=\n"
  "s_waitcnt vmcnt(4)\n"
  "LW16%=:\n"
  "s_waitcnt lgkmcnt(4)\n"
  "v_mfma_f32_32x32x16_bf16 v[8:23], v[128:131], v[112:115], v[8:23]\n"
  "ds_read_b128 v[148:151], v168 offset:5120\n"
  "s_waitcnt lgkmcnt(4)\n"
  "v_mfma_f32_32x32x16_bf16 v[24:39], v[132:135], v[112:115], v[24:39]\n"
  "ds_read_b128 v[152:155], v168 offset:6144\n"
  "s_waitcnt lgkmcnt(4)\n"
  "v_mfma_f32_32x32x16_bf16 v[40:55], v[136:139], v[112:115], v[40:55]\n"
  "ds_read_b128 v[156:159], v168 offset:7168\n"
  "s_waitcnt lgkmcnt(4)\n"
  "v_mfma_f32_32x32x16_bf16 v[56:71], v[140:143], v[112:115], v[56:71]\n"
  "ds_read_b128 v[160:163], v168 offset:8192\n"
  "s_waitcnt lgkmcnt(4)\n"
  "v_mfma_f32_32x32x16_bf16 v[72:87], v[144:147], v[112:115], v[72:87]\n"
  "ds_read_b128 v[164:167], v168 offset:9216\n"
  "s_waitcnt vmcnt(7)\n"
  "s_cbranch_scc0 LW17%=\n"
  "s_waitcnt vmcnt(3)\n"
  "LW17%=:\n"
  "s_waitcnt lgkmcnt(4)\n"
  "v_mfma_f32_32x32x16_bf16 v[8:23], v[148:151], v[116:119], v[8:23]\n"
  "ds_read_b128 v[128:131], v168 offset:10240\n"
  "s_waitcnt lgkmcnt(4)\n"
  "v_mfma_f32_32x32x16_bf16 v[24:39], v[152:155], v[116:119], v[24:39]\n"
  "ds_read_b128 v[132:135], v168 offset:11264\n"
  "s_waitcnt lgkmcnt(4)\n"
  "v_mfma_f32_32x32x16_bf16 v[40:55], v[156:159], v[116:119], v[40:55]\n"
  "ds_read_b128 v[136:139], v168 offset:12288\n"
  "s_waitcnt lgkmcnt(4)\n"
  "v_mfma_f32_32x32x16_bf16 v[56:71], v[160:163], v[116:119], v[56:71]\n"
  "ds_read_b128 v[140:143], v168 offset:13312\n"
  "s_waitcnt lgkmcnt(4)\n"
  "v_mfma_f32_32x32x16_bf16 v[72:87], v[164:167], v[116:119], v[72:87]\n"
  "ds_read_b128 v[144:147], v168 offset:14336\n"
  "s_waitcnt vmcnt(6)\n"
  "s_cbranch_scc0 LW18%=\n"
  "s_waitcnt vmcnt(2)\n"
  "LW18%=:\n"
  "s_waitcnt lgkmcnt(4)\n"
  "v_mfma_f32_32x32x16_bf16 v[8:23], v[128:131], v[120:123], v[8:23]\n"
  "ds_read_b128 v[148:151], v168 offset:15360\n"
  "ds_read_b128 v[152:155], v168 offset:16384\n"
  "s_cbranch_scc1 LS180%=\n"
  "global_load_dwordx4 v[108:111], v169, s[24:25] offset:1024 sc1\n"
  "LS180%=:\n"
  "s_waitcnt lgkmcnt(5)\n"
  "v_mfma_f32_32x32x16_bf16 v[24:39], v[132:135], v[120:123], v[24:39]\n"
  "ds_read_b128 v[156:159], v168 offset:17408\n"
  "ds_read_b128 v[160:163], v168 offset:18432\n"
  "s_cbranch_scc1 LS181%=\n"
  "global_load_dwordx4 v[112:115], v169, s[24:25] offset:2048 sc1\n"
  "LS181%=:\n"
  "s_waitcnt lgkmcnt(6)\n"
  "v_mfma_f32_32x32x16_bf16 v[40:55], v[136:139], v[120:123], v[40:55]\n"
  "ds_read_b128 v[164:167], v168 offset:19456\n"
  "s_cbranch_scc1 LS182%=\n"
  "global_load_dwordx4 v[116:119], v169, s[24:25] offset:3072 sc1\n"
  "v_add_u32 v169, 0x1000, v169\n"
  "LS182%=:\n"
  "ds_read_b128 v[182:185], v170 offset:8192\n"
  "s_waitcnt lgkmcnt(7)\n"
  "v_mfma_f32_32x32x16_bf16 v[56:71], v[140:143], v[120:123], v[56:71]\n"
  "ds_read_b128 v[186:189], v170 offset:9216\n"
  "ds_read_b128 v[190:193], v171\n"
  "ds_read_b128 v[194:197], v171 offset:10240\n"
  "s_waitcnt lgkmcnt(9)\n"
  "v_mfma_f32_32x32x16_bf16 v[72:87], v[144:147], v[120:123], v[72:87]\n"
  "s_cbranch_vccz LR18%=\n"
  "ds_read_b128 v[198:201], v172\n"
  "ds_read_b128 v[202:205], v172 offset:10240\n"
  "LR18%=:\n"
  "s_cbranch_scc1 LS183%=\n"
  "global_load_dwordx4 v[120:123], v169, s[24:25] offset:0 sc1\n"
  "LS183%=:\n"
  "s_waitcnt vmcnt(9)\n"
  "s_cbranch_scc0 LW19%=\n"
  "s_waitcnt vmcnt(1)\n"
  "LW19%=:\n"
  "s_waitcnt lgkmcnt(4)\n"
  "v_mfma_f32_32x32x16_bf16 v[8:23], v[148:151], v[124:127], v[8:23]\n"
  "s_waitcnt lgkmcnt(0)\n"
  "s_cbranch_scc1 LE%=\n"
  "s_barrier\n"
  "v_add_u32 v168, -81920, v168\n"
  "ds_read_b128 v[128:131], v168 offset:0\n"
  "ds_read_b128 v[132:135], v168 offset:1024\n"
  "v_mfma_f32_32x32x16_bf16 v[24:39], v[152:155], v[124:127], v[24:39]\n"
  "ds_read_b128 v[136:139], v168 offset:2048\n"
  "v_add_u32 v171, -81920, v171\n"
  "v_mfma_f32_32x32x16_bf16 v[40:55], v[156:159], v[124:127], v[40:55]\n"
  "ds_read_b128 v[140:143], v168 offset:3072\n"
  "v_add_u32 v172, -81920, v172\n"
  "v_mfma_f32_32x32x16_bf16 v[56:71], v[160:163], v[124:127], v[56:71]\n"
  "ds_read_b128 v[144:147], v168 offset:4096\n"
  "v_mfma_f32_32x32x16_bf16 v[72:87], v[164:167], v[124:127], v[72:87]\n"
  "v_mfma_f32_16x16x32_bf16 v[174:177], v[190:193], v[182:185], v[174:177]\n"
  "v_mfma_f32_16x16x32_bf16 v[174:177], v[194:197], v[186:189], v[174:177]\n"
  "s_cbranch_vccz LH19%=\n"
  "v_mfma_f32_16x16x32_bf16 v[178:181], v[198:201], v[182:185], v[178:181]\n"
  "v_mfma_f32_16x16x32_bf16 v[178:181], v[202:205], v[186:189], v[178:181]\n"
  "LH19%=:\n"
  "v_add_u32 v170, 10240, v170\n"
  "s_mov_b32 s20, 1\n"
  "s_branch LG%=\n"
  "LE%=:\n"
  "v_mfma_f32_32x32x16_bf16 v[24:39], v[152:155], v[124:127], v[24:39]\n"
  "v_mfma_f32_32x32x16_bf16 v[40:55], v[156:159], v[124:127], v[40:55]\n"
  "v_mfma_f32_32x32x16_bf16 v[56:71], v[160:163], v[124:127], v[56:71]\n"
  "v_mfma_f32_32x32x16_bf16 v[72:87], v[164:167], v[124:127], v[72:87]\n"
  "v_mfma_f32_16x16x32_bf16 v[174:177], v[190:193], v[182:185], v[174:177]\n"
  "v_mfma_f32_16x16x32_bf16 v[174:177], v[194:197], v[186:189], v[174:177]\n"
  "v_mfma_f32_16x16x32_bf16 v[178:181], v[198:201], v[182:185], v[178:181]\n"
  "v_mfma_f32_16x16x32_bf16 v[178:181], v[202:205], v[186:189], v[178:181]\n"
  "s_waitcnt vmcnt(0)\n"
  "s_nop 15\n"
    : "+{v[8:23]}"(acc[0]), "+{v[24:39]}"(acc[1]), "+{v[40:55]}"(acc[2]), "+{v[56:71]}"(acc[3]), "+{v[72:87]}"(acc[4]), "+{v[174:177]}"(ha), "+{v[178:181]}"(hb),
      "+{v168}"(wa), "+{v169}"(xo), "+{v170}"(hxa), "+{v171}"(hw0), "+{v172}"(hw1)
    : "{s[24:25]}"(xs), "{s26}"(wid) : "memory", "scc", "vcc", "s20", "v88", "v89", "v90", "v91", "v92", "v93", "v94", "v95", "v96", "v97", "v98", "v99", "v100", "v101", "v102", "v103", "v104", "v105", "v106", "v107", "v108", "v109", "v110", "v111", "v112", "v113", "v114", "v115", "v116", "v117", "v118", "v119", "v120", "v121", "v122", "v123", "v124", "v125", "v126", "v127", "v128", "v129", "v130", "v131", "v132", "v133", "v134", "v135", "v136", "v137", "v138", "v139", "v140", "v141", "v142", "v143", "v144", "v145", "v146", "v147", "v148", "v149", "v150", "v151", "v152", "v153", "v154", "v155", "v156", "v157", "v158", "v159", "v160", "v161", "v162", "v163", "v164", "v165", "v166", "v167", "v182", "v183", "v184", "v185", "v186", "v187", "v188", "v189", "v190", "v191", "v192", "v193", "v194", "v195", "v196", "v197", "v198", "v199", "v200", "v201", "v202", "v203", "v204", "v205");
    auto put = [&](int ht, f32x4 v) { const int n = 2 * ht + (G >> 1);
      *(LDSP u32x2*)(LW + L_BS + ((tk & 1) * 72 + (tk >> 1)) * BS_ROW + (G & 1) * 144 + (n < 4 ? 48 + (n & 1) * 16 + (n >> 1) * 8 : n == 4 ? 80 : 56 + n * 8)) = (u32x2){cvtpk(v[0], v[1]), cvtpk(v[2], v[3])}; };
    if (wid == 1 && lane < 32) *(LDSP u32x2*)(LW + L_BS + ((tk & 1) * 72 + (tk >> 1)) * BS_ROW + G * 144 + 136) = (u32x2){0x3F80u, 0u};
    put(wid + 1, ha); if (wid == 0) put(0, hb);
  }
  {
    LDSP char* rp = LW + L_BS + ((r32 & 1) * 72 + 8 + wid * 16 + (r32 >> 1)) * BS_ROW;
    auto pk = [&](int off, const f32x16& a, int r) { *(LDSP u32x2*)(rp + off) = (u32x2){cvtpk(a[r], a[r + 1]), cvtpk(a[r + 2], a[r + 3])}; };
    if (h) { pk(0, acc[0], 8); pk(144, acc[1], 8); }
    rp += h * 144;
    pk(16, acc[0], 12); pk(8, acc[1], 12); pk(24, acc[2], 0); pk(32, acc[2], 4);
    pk(48, acc[2], 8); pk(64, acc[2], 12); pk(56, acc[3], 0); pk(72, acc[3], 4); pk(80, acc[3], 8);
    pk(96, acc[3], 12); pk(104, acc[4], 0); pk(112, acc[4], 4); pk(120, acc[4], 8); pk(128, acc[4], 12);
    *(LDSP u32x2*)(rp + 136) = (u32x2){0x3F80u, 0u};
  }
  u32x4 qa0, qb0; u32x2 qa1, qb1;
#pragma unroll
  for (int i = 0; i < 4; ++i) { qa0[i] = cvtpk(acc[0][2 * i], acc[0][2 * i + 1]); qb0[i] = cvtpk(acc[1][2 * i], acc[1][2 * i + 1]); }
#pragma unroll
  for (int i = 0; i < 2; ++i) { qa1[i] = h ? 0u : cvtpk(acc[0][8 + 2 * i], acc[0][9 + 2 * i]); qb1[i] = h ? 0u : cvtpk(acc[1][8 + 2 * i], acc[1][9 + 2 * i]); }
  asm volatile("" : "+v"(qa0), "+v"(qb0), "+v"(qa1), "+v"(qb1));
  BARRIER();
  const float gate = *(const LDSP float*)(L + L_G + (tg == 15 ? 512 : 0) + (wid * 32 + r32) * 4);
  const int vrow = r32 < 21 ? r32 : 21;
  const unsigned ak0 = lds0 + L_KVR + lane * 16, ak1 = lds0 + L_KVR + r32 * 16, av = lds0 + L_KVR + (h * 22 + vrow) * 16;
  u32x2 q2b; f32x16 na, nb;
#pragma unroll
  for (int i = 0; i < 2; ++i) q2b[i] = __builtin_amdgcn_permlane32_swap(qa1[i], qb1[i], false, false)[0];
  {
    const bf16x8 k0 = *(const LDSP bf16x8*)(L + L_KVR + lane * 16); const s16x4 k1 = *(const LDSP s16x4*)(L + L_KVR + OFF_K1 + r32 * 16);
    const f32x16 z = {};
    f32x16 sa = MFMA32K8(k1, __builtin_bit_cast(s16x4, qa1), z), sb = MFMA32K8(k1, __builtin_bit_cast(s16x4, qb1), z);
    sa = MFMA32(k0, __builtin_bit_cast(bf16x8, qa0), sa); sb = MFMA32(k0, __builtin_bit_cast(bf16x8, qb0), sb);
    float ma = sa[0], mb = sb[0];
#pragma unroll
    for (int i = 1; i < 16; ++i) { ma = fmaxf(ma, sa[i]); mb = fmaxf(mb, sb[i]); }
    ma = fmaxf(ma, xhalf(ma)); mb = fmaxf(mb, xhalf(mb));
#pragma unroll
    for (int i = 0; i < 16; ++i) { na[i] = -ma; nb[i] = -mb; }
  }
  f32x16 oa, ob;
  asm volatile(
  "ds_read_b128 v[124:127], v153 offset:0\n"
  "ds_read_b128 v[188:191], v154 offset:9728\n"
  "ds_read_b128 v[128:131], v153 offset:2432\n"
  "ds_read_b128 v[144:147], v155 offset:1728\n"
  "ds_read_b128 v[136:139], v155 offset:1024\n"
  "v_mov_b32 v152, 0xdddd\n"
  "v_mov_b32 v16, 0\n"
  "v_mov_b32 v17, 0\n"
  "v_mov_b32 v18, 0\n"
  "v_mov_b32 v19, 0\n"
  "v_mov_b32 v20, 0\n"
  "v_mov_b32 v21, 0\n"
  "v_mov_b32 v22, 0\n"
  "v_mov_b32 v23, 0\n"
  "v_mov_b32 v24, 0\n"
  "v_mov_b32 v25, 0\n"
  "v_mov_b32 v26, 0\n"
  "v_mov_b32 v27, 0\n"
  "v_mov_b32 v28, 0\n"
  "v_mov_b32 v29, 0\n"
  "v_mov_b32 v30, 0\n"
  "v_mov_b32 v31, 0\n"
  "v_mov_b32 v32, 0\n"
  "v_mov_b32 v33, 0\n"
  "v_mov_b32 v34, 0\n"
  "v_mov_b32 v35, 0\n"
  "v_mov_b32 v36, 0\n"
  "v_mov_b32 v37, 0\n"
  "v_mov_b32 v38, 0\n"
  "v_mov_b32 v39, 0\n"
  "v_mov_b32 v40, 0\n"
  "v_mov_b32 v41, 0\n"
  "v_mov_b32 v42, 0\n"
  "v_mov_b32 v43, 0\n"
  "v_mov_b32 v44, 0\n"
  "v_mov_b32 v45, 0\n"
  "v_mov_b32 v46, 0\n"
  "v_mov_b32 v47, 0\n"
  "s_waitcnt lgkmcnt(0)\n"
  "v_mfma_f32_32x32x4_2b_bf16 v[48:79], v[188:189], v[120:121], v[156:187]\n"
  "s_nop 15\n"
  "s_nop 1\n"
  "v_mfma_f32_32x32x16_bf16 v[48:63], v[124:127], v[112:115], v[48:63]\n"
  "v_mfma_f32_32x32x16_bf16 v[64:79], v[124:127], v[116:119], v[64:79]\n"
  "s_nop 11\n"
  "v_exp_f32 v48, v48\n"
  "v_exp_f32 v49, v49\n"
  "v_exp_f32 v50, v50\n"
  "v_exp_f32 v51, v51\n"
  "ds_read_b128 v[124:127], v153 offset:4864\n"
  "ds_read_b128 v[192:195], v154 offset:10240\n"
  "v_exp_f32 v52, v52\n"
  "v_exp_f32 v53, v53\n"
  "v_exp_f32 v54, v54\n"
  "v_exp_f32 v55, v55\n"
  "v_mfma_f32_32x32x4_2b_bf16 v[80:111], v[190:191], v[120:121], v[156:187]\n"
  "v_exp_f32 v56, v56\n"
  "v_exp_f32 v57, v57\n"
  "v_exp_f32 v58, v58\n"
  "v_exp_f32 v59, v59\n"
  "v_exp_f32 v60, v60\n"
  "v_exp_f32 v61, v61\n"
  "v_exp_f32 v62, v62\n"
  "v_exp_f32 v63, v63\n"
  "v_smfmac_f32_32x32x32_bf16 v[16:31], v[136:139], v[48:55], v152\n"
  "ds_read_b128 v[148:151], v155 offset:4160\n"
  "ds_read_b128 v[140:143], v155 offset:3456\n"
  "v_exp_f32 v64, v64\n"
  "v_exp_f32 v65, v65\n"
  "v_exp_f32 v66, v66\n"
  "v_exp_f32 v67, v67\n"
  "v_smfmac_f32_32x32x32_bf16 v[16:31], v[144:147], v[56:63], v152\n"
  "v_exp_f32 v68, v68\n"
  "v_exp_f32 v69, v69\n"
  "v_exp_f32 v70, v70\n"
  "v_exp_f32 v71, v71\n"
  "v_mfma_f32_32x32x16_bf16 v[80:95], v[128:131], v[112:115], v[80:95]\n"
  "v_exp_f32 v72, v72\n"
  "v_exp_f32 v73, v73\n"
  "v_exp_f32 v74, v74\n"
  "v_exp_f32 v75, v75\n"
  "v_mfma_f32_32x32x16_bf16 v[96:111], v[128:131], v[116:119], v[96:111]\n"
  "v_exp_f32 v76, v76\n"
  "v_exp_f32 v77, v77\n"
  "v_exp_f32 v78, v78\n"
  "v_exp_f32 v79, v79\n"
  "v_smfmac_f32_32x32x32_bf16 v[32:47], v[136:139], v[64:71], v152\n"
  "s_waitcnt lgkmcnt(0)\n"
  "v_exp_f32 v80, v80\n"
  "v_exp_f32 v81, v81\n"
  "v_exp_f32 v82, v82\n"
  "v_exp_f32 v83, v83\n"
  "v_smfmac_f32_32x32x32_bf16 v[32:47], v[144:147], v[72:79], v152\n"
  "ds_read_b128 v[128:131], v153 offset:7296\n"
  "v_exp_f32 v84, v84\n"
  "v_exp_f32 v85, v85\n"
  "v_exp_f32 v86, v86\n"
  "v_exp_f32 v87, v87\n"
  "v_mfma_f32_32x32x4_2b_bf16 v[48:79], v[192:193], v[120:121], v[156:187]\n"
  "v_exp_f32 v88, v88\n"
  "v_exp_f32 v89, v89\n"
  "v_exp_f32 v90, v90\n"
  "v_exp_f32 v91, v91\n"
  "v_exp_f32 v92, v92\n"
  "v_exp_f32 v93, v93\n"
  "v_exp_f32 v94, v94\n"
  "v_exp_f32 v95, v95\n"
  "v_smfmac_f32_32x32x32_bf16 v[16:31], v[140:143], v[80:87], v152\n"
  "ds_read_b128 v[144:147], v155 offset:6592\n"
  "ds_read_b128 v[136:139], v155 offset:5888\n"
  "v_exp_f32 v96, v96\n"
  "v_exp_f32 v97, v97\n"
  "v_exp_f32 v98, v98\n"
  "v_exp_f32 v99, v99\n"
  "v_smfmac_f32_32x32x32_bf16 v[16:31], v[148:151], v[88:95], v152\n"
  "v_exp_f32 v100, v100\n"
  "v_exp_f32 v101, v101\n"
  "v_exp_f32 v102, v102\n"
  "v_exp_f32 v103, v103\n"
  "v_mfma_f32_32x32x16_bf16 v[48:63], v[124:127], v[112:115], v[48:63]\n"
  "v_exp_f32 v104, v104\n"
  "v_exp_f32 v105, v105\n"
  "v_exp_f32 v106, v106\n"
  "v_exp_f32 v107, v107\n"
  "v_mfma_f32_32x32x16_bf16 v[64:79], v[124:127], v[116:119], v[64:79]\n"
  "v_exp_f32 v108, v108\n"
  "v_exp_f32 v109, v109\n"
  "v_exp_f32 v110, v110\n"
  "v_exp_f32 v111, v111\n"
  "v_smfmac_f32_32x32x32_bf16 v[32:47], v[140:143], v[96:103], v152\n"
  "s_waitcnt lgkmcnt(0)\n"
  "s_mov_b32 s20, 5\n"
  "LATT%=:\n"
  "v_exp_f32 v48, v48\n"
  "v_exp_f32 v49, v49\n"
  "v_exp_f32 v50, v50\n"
  "v_exp_f32 v51, v51\n"
  "v_smfmac_f32_32x32x32_bf16 v[32:47], v[148:151], v[104:111], v152\n"
  "ds_read_b128 v[124:127], v153 offset:11264\n"
  "ds_read_b128 v[188:191], v154 offset:20992\n"
  "v_exp_f32 v52, v52\n"
  "v_exp_f32 v53, v53\n"
  "v_exp_f32 v54, v54\n"
  "v_exp_f32 v55, v55\n"
  "v_mfma_f32_32x32x4_2b_bf16 v[80:111], v[194:195], v[120:121], v[156:187]\n"
  "v_exp_f32 v56, v56\n"
  "v_exp_f32 v57, v57\n"
  "v_exp_f32 v58, v58\n"
  "v_exp_f32 v59, v59\n"
  "v_exp_f32 v60, v60\n"
  "v_exp_f32 v61, v61\n"
  "v_exp_f32 v62, v62\n"
  "v_exp_f32 v63, v63\n"
  "v_smfmac_f32_32x32x32_bf16 v[16:31], v[136:139], v[48:55], v152\n"
  "ds_read_b128 v[148:151], v155 offset:9024\n"
  "ds_read_b128 v[140:143], v155 offset:8320\n"
  "v_exp_f32 v64, v64\n"
  "v_exp_f32 v65, v65\n"
  "v_exp_f32 v66, v66\n"
  "v_exp_f32 v67, v67\n"
  "v_smfmac_f32_32x32x32_bf16 v[16:31], v[144:147], v[56:63], v152\n"
  "v_exp_f32 v68, v68\n"
  "v_exp_f32 v69, v69\n"
  "v_exp_f32 v70, v70\n"
  "v_exp_f32 v71, v71\n"
  "v_mfma_f32_32x32x16_bf16 v[80:95], v[128:131], v[112:115], v[80:95]\n"
  "v_exp_f32 v72, v72\n"
  "v_exp_f32 v73, v73\n"
  "v_exp_f32 v74, v74\n"
  "v_exp_f32 v75, v75\n"
  "v_mfma_f32_32x32x16_bf16 v[96:111], v[128:131], v[116:119], v[96:111]\n"
  "v_exp_f32 v76, v76\n"
  "v_exp_f32 v77, v77\n"
  "v_exp_f32 v78, v78\n"
  "v_exp_f32 v79, v79\n"
  "v_smfmac_f32_32x32x32_bf16 v[32:47], v[136:139], v[64:71], v152\n"
  "s_waitcnt lgkmcnt(0)\n"
  "v_exp_f32 v80, v80\n"
  "v_exp_f32 v81, v81\n"
  "v_exp_f32 v82, v82\n"
  "v_exp_f32 v83, v83\n"
  "v_smfmac_f32_32x32x32_bf16 v[32:47], v[144:147], v[72:79], v152\n"
  "ds_read_b128 v[128:131], v153 offset:13696\n"
  "v_exp_f32 v84, v84\n"
  "v_exp_f32 v85, v85\n"
  "v_exp_f32 v86, v86\n"
  "v_exp_f32 v87, v87\n"
  "v_mfma_f32_32x32x4_2b_bf16 v[48:79], v[188:189], v[120:121], v[156:187]\n"
  "v_exp_f32 v88, v88\n"
  "v_exp_f32 v89, v89\n"
  "v_exp_f32 v90, v90\n"
  "v_exp_f32 v91, v91\n"
  "v_exp_f32 v92, v92\n"
  "v_exp_f32 v93, v93\n"
  "v_exp_f32 v94, v94\n"
  "v_exp_f32 v95, v95\n"
  "v_smfmac_f32_32x32x32_bf16 v[16:31], v[140:143], v[80:87], v152\n"
  "ds_read_b128 v[144:147], v155 offset:12992\n"
  "ds_read_b128 v[136:139], v155 offset:12288\n"
  "v_exp_f32 v96, v96\n"
  "v_exp_f32 v97, v97\n"
  "v_exp_f32 v98, v98\n"
  "v_exp_f32 v99, v99\n"
  "v_smfmac_f32_32x32x32_bf16 v[16:31], v[148:151], v[88:95], v152\n"
  "v_exp_f32 v100, v100\n"
  "v_exp_f32 v101, v101\n"
  "v_exp_f32 v102, v102\n"
  "v_exp_f32 v103, v103\n"
  "v_mfma_f32_32x32x16_bf16 v[48:63], v[124:127], v[112:115], v[48:63]\n"
  "v_exp_f32 v104, v104\n"
  "v_exp_f32 v105, v105\n"
  "v_exp_f32 v106, v106\n"
  "v_exp_f32 v107, v107\n"
  "v_mfma_f32_32x32x16_bf16 v[64:79], v[124:127], v[116:119], v[64:79]\n"
  "v_exp_f32 v108, v108\n"
  "v_exp_f32 v109, v109\n"
  "v_exp_f32 v110, v110\n"
  "v_exp_f32 v111, v111\n"
  "v_smfmac_f32_32x32x32_bf16 v[32:47], v[140:143], v[96:103], v152\n"
  "s_waitcnt lgkmcnt(0)\n"
  "v_exp_f32 v48, v48\n"
  "v_exp_f32 v49, v49\n"
  "v_exp_f32 v50, v50\n"
  "v_exp_f32 v51, v51\n"
  "v_smfmac_f32_32x32x32_bf16 v[32:47], v[148:151], v[104:111], v152\n"
  "ds_read_b128 v[124:127], v153 offset:16128\n"
  "ds_read_b128 v[192:195], v154 offset:21504\n"
  "v_exp_f32 v52, v52\n"
  "v_exp_f32 v53, v53\n"
  "v_exp_f32 v54, v54\n"
  "v_exp_f32 v55, v55\n"
  "v_mfma_f32_32x32x4_2b_bf16 v[80:111], v[190:191], v[120:121], v[156:187]\n"
  "v_exp_f32 v56, v56\n"
  "v_exp_f32 v57, v57\n"
  "v_exp_f32 v58, v58\n"
  "v_exp_f32 v59, v59\n"
  "v_exp_f32 v60, v60\n"
  "v_exp_f32 v61, v61\n"
  "v_exp_f32 v62, v62\n"
  "v_exp_f32 v63, v63\n"
  "v_smfmac_f32_32x32x32_bf16 v[16:31], v[136:139], v[48:55], v152\n"
  "ds_read_b128 v[148:151], v155 offset:15424\n"
  "ds_read_b128 v[140:143], v155 offset:14720\n"
  "v_exp_f32 v64, v64\n"
  "v_exp_f32 v65, v65\n"
  "v_exp_f32 v66, v66\n"
  "v_exp_f32 v67, v67\n"
  "v_smfmac_f32_32x32x32_bf16 v[16:31], v[144:147], v[56:63], v152\n"
  "v_exp_f32 v68, v68\n"
  "v_exp_f32 v69, v69\n"
  "v_exp_f32 v70, v70\n"
  "v_exp_f32 v71, v71\n"
  "v_mfma_f32_32x32x16_bf16 v[80:95], v[128:131], v[112:115], v[80:95]\n"
  "v_exp_f32 v72, v72\n"
  "v_exp_f32 v73, v73\n"
  "v_exp_f32 v74, v74\n"
  "v_exp_f32 v75, v75\n"
  "v_mfma_f32_32x32x16_bf16 v[96:111], v[128:131], v[116:119], v[96:111]\n"
  "v_exp_f32 v76, v76\n"
  "v_exp_f32 v77, v77\n"
  "v_exp_f32 v78, v78\n"
  "v_exp_f32 v79, v79\n"
  "v_smfmac_f32_32x32x32_bf16 v[32:47], v[136:139], v[64:71], v152\n"
  "s_waitcnt lgkmcnt(0)\n"
  "v_exp_f32 v80, v80\n"
  "v_exp_f32 v81, v81\n"
  "v_exp_f32 v82, v82\n"
  "v_exp_f32 v83, v83\n"
  "v_smfmac_f32_32x32x32_bf16 v[32:47], v[144:147], v[72:79], v152\n"
  "ds_read_b128 v[128:131], v153 offset:18560\n"
  "v_exp_f32 v84, v84\n"
  "v_exp_f32 v85, v85\n"
  "v_exp_f32 v86, v86\n"
  "v_exp_f32 v87, v87\n"
  "v_mfma_f32_32x32x4_2b_bf16 v[48:79], v[192:193], v[120:121], v[156:187]\n"
  "v_exp_f32 v88, v88\n"
  "v_exp_f32 v89, v89\n"
  "v_exp_f32 v90, v90\n"
  "v_exp_f32 v91, v91\n"
  "v_exp_f32 v92, v92\n"
  "v_exp_f32 v93, v93\n"
  "v_exp_f32 v94, v94\n"
  "v_exp_f32 v95, v95\n"
  "v_smfmac_f32_32x32x32_bf16 v[16:31], v[140:143], v[80:87], v152\n"
  "ds_read_b128 v[144:147], v155 offset:17856\n"
  "ds_read_b128 v[136:139], v155 offset:17152\n"
  "v_exp_f32 v96, v96\n"
  "v_exp_f32 v97, v97\n"
  "v_exp_f32 v98, v98\n"
  "v_exp_f32 v99, v99\n"
  "v_smfmac_f32_32x32x32_bf16 v[16:31], v[148:151], v[88:95], v152\n"
  "s_barrier\n"
  "v_exp_f32 v100, v100\n"
  "v_exp_f32 v101, v101\n"
  "v_exp_f32 v102, v102\n"
  "v_exp_f32 v103, v103\n"
  "v_mfma_f32_32x32x16_bf16 v[48:63], v[124:127], v[112:115], v[48:63]\n"
  "v_exp_f32 v104, v104\n"
  "v_exp_f32 v105, v105\n"
  "v_exp_f32 v106, v106\n"
  "v_exp_f32 v107, v107\n"
  "v_mfma_f32_32x32x16_bf16 v[64:79], v[124:127], v[116:119], v[64:79]\n"
  "v_exp_f32 v108, v108\n"
  "v_exp_f32 v109, v109\n"
  "v_exp_f32 v110, v110\n"
  "v_exp_f32 v111, v111\n"
  "v_smfmac_f32_32x32x32_bf16 v[32:47], v[140:143], v[96:103], v152\n"
  "s_waitcnt lgkmcnt(0)\n"
  "v_exp_f32 v48, v48\n"
  "v_exp_f32 v49, v49\n"
  "v_exp_f32 v50, v50\n"
  "v_exp_f32 v51, v51\n"
  "v_smfmac_f32_32x32x32_bf16 v[32:47], v[148:151], v[104:111], v152\n"
  "ds_read_b128 v[124:127], v153 offset:22528\n"
  "ds_read_b128 v[188:191], v154 offset:32256\n"
  "v_exp_f32 v52, v52\n"
  "v_exp_f32 v53, v53\n"
  "v_exp_f32 v54, v54\n"
  "v_exp_f32 v55, v55\n"
  "v_mfma_f32_32x32x4_2b_bf16 v[80:111], v[194:195], v[120:121], v[156:187]\n"
  "v_exp_f32 v56, v56\n"
  "v_exp_f32 v57, v57\n"
  "v_exp_f32 v58, v58\n"
  "v_exp_f32 v59, v59\n"
  "v_exp_f32 v60, v60\n"
  "v_exp_f32 v61, v61\n"
  "v_exp_f32 v62, v62\n"
  "v_exp_f32 v63, v63\n"
  "v_smfmac_f32_32x32x32_bf16 v[16:31], v[136:139], v[48:55], v152\n"
  "ds_read_b128 v[148:151], v155 offset:20288\n"
  "ds_read_b128 v[140:143], v155 offset:19584\n"
  "v_exp_f32 v64, v64\n"
  "v_exp_f32 v65, v65\n"
  "v_exp_f32 v66, v66\n"
  "v_exp_f32 v67, v67\n"
  "v_smfmac_f32_32x32x32_bf16 v[16:31], v[144:147], v[56:63], v152\n"
  "v_exp_f32 v68, v68\n"
  "v_exp_f32 v69, v69\n"
  "v_exp_f32 v70, v70\n"
  "v_exp_f32 v71, v71\n"
  "v_mfma_f32_32x32x16_bf16 v[80:95], v[128:131], v[112:115], v[80:95]\n"
  "v_exp_f32 v72, v72\n"
  "v_exp_f32 v73, v73\n"
  "v_exp_f32 v74, v74\n"
  "v_exp_f32 v75, v75\n"
  "v_mfma_f32_32x32x16_bf16 v[96:111], v[128:131], v[116:119], v[96:111]\n"
  "v_exp_f32 v76, v76\n"
  "v_exp_f32 v77, v77\n"
  "v_exp_f32 v78, v78\n"
  "v_exp_f32 v79, v79\n"
  "v_smfmac_f32_32x32x32_bf16 v[32:47], v[136:139], v[64:71], v152\n"
  "s_waitcnt lgkmcnt(0)\n"
  "v_exp_f32 v80, v80\n"
  "v_exp_f32 v81, v81\n"
  "v_exp_f32 v82, v82\n"
  "v_exp_f32 v83, v83\n"
  "v_smfmac_f32_32x32x32_bf16 v[32:47], v[144:147], v[72:79], v152\n"
  "ds_read_b128 v[128:131], v153 offset:24960\n"
  "v_exp_f32 v84, v84\n"
  "v_exp_f32 v85, v85\n"
  "v_exp_f32 v86, v86\n"
  "v_exp_f32 v87, v87\n"
  "v_mfma_f32_32x32x4_2b_bf16 v[48:79], v[188:189], v[120:121], v[156:187]\n"
  "v_exp_f32 v88, v88\n"
  "v_exp_f32 v89, v89\n"
  "v_exp_f32 v90, v90\n"
  "v_exp_f32 v91, v91\n"
  "v_exp_f32 v92, v92\n"
  "v_exp_f32 v93, v93\n"
  "v_exp_f32 v94, v94\n"
  "v_exp_f32 v95, v95\n"
  "v_smfmac_f32_32x32x32_bf16 v[16:31], v[140:143], v[80:87], v152\n"
  "ds_read_b128 v[144:147], v155 offset:24256\n"
  "ds_read_b128 v[136:139], v155 offset:23552\n"
  "v_exp_f32 v96, v96\n"
  "v_exp_f32 v97, v97\n"
  "v_exp_f32 v98, v98\n"
  "v_exp_f32 v99, v99\n"
  "v_smfmac_f32_32x32x32_bf16 v[16:31], v[148:151], v[88:95], v152\n"
  "v_exp_f32 v100, v100\n"
  "v_exp_f32 v101, v101\n"
  "v_exp_f32 v102, v102\n"
  "v_exp_f32 v103, v103\n"
  "v_mfma_f32_32x32x16_bf16 v[48:63], v[124:127], v[112:115], v[48:63]\n"
  "v_exp_f32 v104, v104\n"
  "v_exp_f32 v105, v105\n"
  "v_exp_f32 v106, v106\n"
  "v_exp_f32 v107, v107\n"
  "v_mfma_f32_32x32x16_bf16 v[64:79], v[124:127], v[116:119], v[64:79]\n"
  "v_exp_f32 v108, v108\n"
  "v_exp_f32 v109, v109\n"
  "v_exp_f32 v110, v110\n"
  "v_exp_f32 v111, v111\n"
  "v_smfmac_f32_32x32x32_bf16 v[32:47], v[140:143], v[96:103], v152\n"
  "s_waitcnt lgkmcnt(0)\n"
  "v_exp_f32 v48, v48\n"
  "v_exp_f32 v49, v49\n"
  "v_exp_f32 v50, v50\n"
  "v_exp_f32 v51, v51\n"
  "v_smfmac_f32_32x32x32_bf16 v[32:47], v[148:151], v[104:111], v152\n"
  "ds_read_b128 v[124:127], v153 offset:27392\n"
  "ds_read_b128 v[192:195], v154 offset:32768\n"
  "v_exp_f32 v52, v52\n"
  "v_exp_f32 v53, v53\n"
  "v_exp_f32 v54, v54\n"
  "v_exp_f32 v55, v55\n"
  "v_mfma_f32_32x32x4_2b_bf16 v[80:111], v[190:191], v[120:121], v[156:187]\n"
  "v_exp_f32 v56, v56\n"
  "v_exp_f32 v57, v57\n"
  "v_exp_f32 v58, v58\n"
  "v_exp_f32 v59, v59\n"
  "v_exp_f32 v60, v60\n"
  "v_exp_f32 v61, v61\n"
  "v_exp_f32 v62, v62\n"
  "v_exp_f32 v63, v63\n"
  "v_smfmac_f32_32x32x32_bf16 v[16:31], v[136:139], v[48:55], v152\n"
  "ds_read_b128 v[148:151], v155 offset:26688\n"
  "ds_read_b128 v[140:143], v155 offset:25984\n"
  "v_exp_f32 v64, v64\n"
  "v_exp_f32 v65, v65\n"
  "v_exp_f32 v66, v66\n"
  "v_exp_f32 v67, v67\n"
  "v_smfmac_f32_32x32x32_bf16 v[16:31], v[144:147], v[56:63], v152\n"
  "v_exp_f32 v68, v68\n"
  "v_exp_f32 v69, v69\n"
  "v_exp_f32 v70, v70\n"
  "v_exp_f32 v71, v71\n"
  "v_mfma_f32_32x32x16_bf16 v[80:95], v[128:131], v[112:115], v[80:95]\n"
  "v_exp_f32 v72, v72\n"
  "v_exp_f32 v73, v73\n"
  "v_exp_f32 v74, v74\n"
  "v_exp_f32 v75, v75\n"
  "v_mfma_f32_32x32x16_bf16 v[96:111], v[128:131], v[116:119], v[96:111]\n"
  "v_exp_f32 v76, v76\n"
  "v_exp_f32 v77, v77\n"
  "v_exp_f32 v78, v78\n"
  "v_exp_f32 v79, v79\n"
  "v_smfmac_f32_32x32x32_bf16 v[32:47], v[136:139], v[64:71], v152\n"
  "s_waitcnt lgkmcnt(0)\n"
  "v_exp_f32 v80, v80\n"
  "v_exp_f32 v81, v81\n"
  "v_exp_f32 v82, v82\n"
  "v_exp_f32 v83, v83\n"
  "v_smfmac_f32_32x32x32_bf16 v[32:47], v[144:147], v[72:79], v152\n"
  "ds_read_b128 v[128:131], v153 offset:29824\n"
  "v_exp_f32 v84, v84\n"
  "v_exp_f32 v85, v85\n"
  "v_exp_f32 v86, v86\n"
  "v_exp_f32 v87, v87\n"
  "v_mfma_f32_32x32x4_2b_bf16 v[48:79], v[192:193], v[120:121], v[156:187]\n"
  "v_exp_f32 v88, v88\n"
  "v_exp_f32 v89, v89\n"
  "v_exp_f32 v90, v90\n"
  "v_exp_f32 v91, v91\n"
  "v_exp_f32 v92, v92\n"
  "v_exp_f32 v93, v93\n"
  "v_exp_f32 v94, v94\n"
  "v_exp_f32 v95, v95\n"
  "v_smfmac_f32_32x32x32_bf16 v[16:31], v[140:143], v[80:87], v152\n"
  "ds_read_b128 v[144:147], v155 offset:29120\n"
  "ds_read_b128 v[136:139], v155 offset:28416\n"
  "v_exp_f32 v96, v96\n"
  "v_exp_f32 v97, v97\n"
  "v_exp_f32 v98, v98\n"
  "v_exp_f32 v99, v99\n"
  "v_smfmac_f32_32x32x32_bf16 v[16:31], v[148:151], v[88:95], v152\n"
  "s_barrier\n"
  "v_exp_f32 v100, v100\n"
  "v_exp_f32 v101, v101\n"
  "v_exp_f32 v102, v102\n"
  "v_exp_f32 v103, v103\n"
  "v_mfma_f32_32x32x16_bf16 v[48:63], v[124:127], v[112:115], v[48:63]\n"
  "v_exp_f32 v104, v104\n"
  "v_exp_f32 v105, v105\n"
  "v_exp_f32 v106, v106\n"
  "v_exp_f32 v107, v107\n"
  "v_mfma_f32_32x32x16_bf16 v[64:79], v[124:127], v[116:119], v[64:79]\n"
  "v_exp_f32 v108, v108\n"
  "v_exp_f32 v109, v109\n"
  "v_exp_f32 v110, v110\n"
  "v_exp_f32 v111, v111\n"
  "v_smfmac_f32_32x32x32_bf16 v[32:47], v[140:143], v[96:103], v152\n"
  "s_waitcnt lgkmcnt(0)\n"
  "v_exp_f32 v48, v48\n"
  "v_exp_f32 v49, v49\n"
  "v_exp_f32 v50, v50\n"
  "v_exp_f32 v51, v51\n"
  "v_smfmac_f32_32x32x32_bf16 v[32:47], v[148:151], v[104:111], v152\n"
  "ds_read_b128 v[124:127], v153 offset:0\n"
  "ds_read_b128 v[188:191], v154 offset:9728\n"
  "v_exp_f32 v52, v52\n"
  "v_exp_f32 v53, v53\n"
  "v_exp_f32 v54, v54\n"
  "v_exp_f32 v55, v55\n"
  "v_mfma_f32_32x32x4_2b_bf16 v[80:111], v[194:195], v[120:121], v[156:187]\n"
  "v_exp_f32 v56, v56\n"
  "v_exp_f32 v57, v57\n"
  "v_exp_f32 v58, v58\n"
  "v_exp_f32 v59, v59\n"
  "v_exp_f32 v60, v60\n"
  "v_exp_f32 v61, v61\n"
  "v_exp_f32 v62, v62\n"
  "v_exp_f32 v63, v63\n"
  "v_smfmac_f32_32x32x32_bf16 v[16:31], v[136:139], v[48:55], v152\n"
  "ds_read_b128 v[148:151], v155 offset:31552\n"
  "ds_read_b128 v[140:143], v155 offset:30848\n"
  "v_exp_f32 v64, v64\n"
  "v_exp_f32 v65, v65\n"
  "v_exp_f32 v66, v66\n"
  "v_exp_f32 v67, v67\n"
  "v_smfmac_f32_32x32x32_bf16 v[16:31], v[144:147], v[56:63], v152\n"
  "v_exp_f32 v68, v68\n"
  "v_exp_f32 v69, v69\n"
  "v_exp_f32 v70, v70\n"
  "v_exp_f32 v71, v71\n"
  "v_mfma_f32_32x32x16_bf16 v[80:95], v[128:131], v[112:115], v[80:95]\n"
  "v_exp_f32 v72, v72\n"
  "v_exp_f32 v73, v73\n"
  "v_exp_f32 v74, v74\n"
  "v_exp_f32 v75, v75\n"
  "v_mfma_f32_32x32x16_bf16 v[96:111], v[128:131], v[116:119], v[96:111]\n"
  "v_exp_f32 v76, v76\n"
  "v_exp_f32 v77, v77\n"
  "v_exp_f32 v78, v78\n"
  "v_exp_f32 v79, v79\n"
  "v_smfmac_f32_32x32x32_bf16 v[32:47], v[136:139], v[64:71], v152\n"
  "s_waitcnt lgkmcnt(0)\n"
  "v_exp_f32 v80, v80\n"
  "v_exp_f32 v81, v81\n"
  "v_exp_f32 v82, v82\n"
  "v_exp_f32 v83, v83\n"
  "v_smfmac_f32_32x32x32_bf16 v[32:47], v[144:147], v[72:79], v152\n"
  "ds_read_b128 v[128:131], v153 offset:2432\n"
  "v_exp_f32 v84, v84\n"
  "v_exp_f32 v85, v85\n"
  "v_exp_f32 v86, v86\n"
  "v_exp_f32 v87, v87\n"
  "v_mfma_f32_32x32x4_2b_bf16 v[48:79], v[188:189], v[120:121], v[156:187]\n"
  "v_exp_f32 v88, v88\n"
  "v_exp_f32 v89, v89\n"
  "v_exp_f32 v90, v90\n"
  "v_exp_f32 v91, v91\n"
  "v_exp_f32 v92, v92\n"
  "v_exp_f32 v93, v93\n"
  "v_exp_f32 v94, v94\n"
  "v_exp_f32 v95, v95\n"
  "v_smfmac_f32_32x32x32_bf16 v[16:31], v[140:143], v[80:87], v152\n"
  "ds_read_b128 v[144:147], v155 offset:1728\n"
  "ds_read_b128 v[136:139], v155 offset:1024\n"
  "v_exp_f32 v96, v96\n"
  "v_exp_f32 v97, v97\n"
  "v_exp_f32 v98, v98\n"
  "v_exp_f32 v99, v99\n"
  "v_smfmac_f32_32x32x32_bf16 v[16:31], v[148:151], v[88:95], v152\n"
  "v_exp_f32 v100, v100\n"
  "v_exp_f32 v101, v101\n"
  "v_exp_f32 v102, v102\n"
  "v_exp_f32 v103, v103\n"
  "v_mfma_f32_32x32x16_bf16 v[48:63], v[124:127], v[112:115], v[48:63]\n"
  "v_exp_f32 v104, v104\n"
  "v_exp_f32 v105, v105\n"
  "v_exp_f32 v106, v106\n"
  "v_exp_f32 v107, v107\n"
  "v_mfma_f32_32x32x16_bf16 v[64:79], v[124:127], v[116:119], v[64:79]\n"
  "v_exp_f32 v108, v108\n"
  "v_exp_f32 v109, v109\n"
  "v_exp_f32 v110, v110\n"
  "v_exp_f32 v111, v111\n"
  "v_smfmac_f32_32x32x32_bf16 v[32:47], v[140:143], v[96:103], v152\n"
  "s_waitcnt lgkmcnt(0)\n"
  "v_exp_f32 v48, v48\n"
  "v_exp_f32 v49, v49\n"
  "v_exp_f32 v50, v50\n"
  "v_exp_f32 v51, v51\n"
  "v_smfmac_f32_32x32x32_bf16 v[32:47], v[148:151], v[104:111], v152\n"
  "ds_read_b128 v[124:127], v153 offset:4864\n"
  "ds_read_b128 v[192:195], v154 offset:10240\n"
  "v_exp_f32 v52, v52\n"
  "v_exp_f32 v53, v53\n"
  "v_exp_f32 v54, v54\n"
  "v_exp_f32 v55, v55\n"
  "v_mfma_f32_32x32x4_2b_bf16 v[80:111], v[190:191], v[120:121], v[156:187]\n"
  "v_exp_f32 v56, v56\n"
  "v_exp_f32 v57, v57\n"
  "v_exp_f32 v58, v58\n"
  "v_exp_f32 v59, v59\n"
  "v_exp_f32 v60, v60\n"
  "v_exp_f32 v61, v61\n"
  "v_exp_f32 v62, v62\n"
  "v_exp_f32 v63, v63\n"
  "v_smfmac_f32_32x32x32_bf16 v[16:31], v[136:139], v[48:55], v152\n"
  "ds_read_b128 v[148:151], v155 offset:4160\n"
  "ds_read_b128 v[140:143], v155 offset:3456\n"
  "v_exp_f32 v64, v64\n"
  "v_exp_f32 v65, v65\n"
  "v_exp_f32 v66, v66\n"
  "v_exp_f32 v67, v67\n"
  "v_smfmac_f32_32x32x32_bf16 v[16:31], v[144:147], v[56:63], v152\n"
  "v_exp_f32 v68, v68\n"
  "v_exp_f32 v69, v69\n"
  "v_exp_f32 v70, v70\n"
  "v_exp_f32 v71, v71\n"
  "v_mfma_f32_32x32x16_bf16 v[80:95], v[128:131], v[112:115], v[80:95]\n"
  "v_exp_f32 v72, v72\n"
  "v_exp_f32 v73, v73\n"
  "v_exp_f32 v74, v74\n"
  "v_exp_f32 v75, v75\n"
  "v_mfma_f32_32x32x16_bf16 v[96:111], v[128:131], v[116:119], v[96:111]\n"
  "v_exp_f32 v76, v76\n"
  "v_exp_f32 v77, v77\n"
  "v_exp_f32 v78, v78\n"
  "v_exp_f32 v79, v79\n"
  "v_smfmac_f32_32x32x32_bf16 v[32:47], v[136:139], v[64:71], v152\n"
  "s_waitcnt lgkmcnt(0)\n"
  "v_exp_f32 v80, v80\n"
  "v_exp_f32 v81, v81\n"
  "v_exp_f32 v82, v82\n"
  "v_exp_f32 v83, v83\n"
  "v_smfmac_f32_32x32x32_bf16 v[32:47], v[144:147], v[72:79], v152\n"
  "ds_read_b128 v[128:131], v153 offset:7296\n"
  "v_exp_f32 v84, v84\n"
  "v_exp_f32 v85, v85\n"
  "v_exp_f32 v86, v86\n"
  "v_exp_f32 v87, v87\n"
  "v_mfma_f32_32x32x4_2b_bf16 v[48:79], v[192:193], v[120:121], v[156:187]\n"
  "v_exp_f32 v88, v88\n"
  "v_exp_f32 v89, v89\n"
  "v_exp_f32 v90, v90\n"
  "v_exp_f32 v91, v91\n"
  "v_exp_f32 v92, v92\n"
  "v_exp_f32 v93, v93\n"
  "v_exp_f32 v94, v94\n"
  "v_exp_f32 v95, v95\n"
  "v_smfmac_f32_32x32x32_bf16 v[16:31], v[140:143], v[80:87], v152\n"
  "ds_read_b128 v[144:147], v155 offset:6592\n"
  "ds_read_b128 v[136:139], v155 offset:5888\n"
  "v_exp_f32 v96, v96\n"
  "v_exp_f32 v97, v97\n"
  "v_exp_f32 v98, v98\n"
  "v_exp_f32 v99, v99\n"
  "v_smfmac_f32_32x32x32_bf16 v[16:31], v[148:151], v[88:95], v152\n"
  "s_barrier\n"
  "v_exp_f32 v100, v100\n"
  "v_exp_f32 v101, v101\n"
  "v_exp_f32 v102, v102\n"
  "v_exp_f32 v103, v103\n"
  "v_mfma_f32_32x32x16_bf16 v[48:63], v[124:127], v[112:115], v[48:63]\n"
  "v_exp_f32 v104, v104\n"
  "v_exp_f32 v105, v105\n"
  "v_exp_f32 v106, v106\n"
  "v_exp_f32 v107, v107\n"
  "v_mfma_f32_32x32x16_bf16 v[64:79], v[124:127], v[116:119], v[64:79]\n"
  "v_exp_f32 v108, v108\n"
  "v_exp_f32 v109, v109\n"
  "v_exp_f32 v110, v110\n"
  "v_exp_f32 v111, v111\n"
  "v_smfmac_f32_32x32x32_bf16 v[32:47], v[140:143], v[96:103], v152\n"
  "s_waitcnt lgkmcnt(0)\n"
  "s_sub_u32 s20, s20, 1\n"
  "s_cmp_lg_u32 s20, 0\n"
  "s_cbranch_scc1 LATT%=\n"
  "v_exp_f32 v48, v48\n"
  "v_exp_f32 v49, v49\n"
  "v_exp_f32 v50, v50\n"
  "v_exp_f32 v51, v51\n"
  "v_smfmac_f32_32x32x32_bf16 v[32:47], v[148:151], v[104:111], v152\n"
  "v_exp_f32 v52, v52\n"
  "v_exp_f32 v53, v53\n"
  "v_exp_f32 v54, v54\n"
  "v_exp_f32 v55, v55\n"
  "v_mfma_f32_32x32x4_2b_bf16 v[80:111], v[194:195], v[120:121], v[156:187]\n"
  "v_exp_f32 v56, v56\n"
  "v_exp_f32 v57, v57\n"
  "v_exp_f32 v58, v58\n"
  "v_exp_f32 v59, v59\n"
  "v_exp_f32 v60, v60\n"
  "v_exp_f32 v61, v61\n"
  "v_exp_f32 v62, v62\n"
  "v_exp_f32 v63, v63\n"
  "v_smfmac_f32_32x32x32_bf16 v[16:31], v[136:139], v[48:55], v152\n"
  "ds_read_b128 v[148:151], v155 offset:9024\n"
  "ds_read_b128 v[140:143], v155 offset:8320\n"
  "v_exp_f32 v64, v64\n"
  "v_exp_f32 v65, v65\n"
  "v_exp_f32 v66, v66\n"
  "v_exp_f32 v67, v67\n"
  "v_smfmac_f32_32x32x32_bf16 v[16:31], v[144:147], v[56:63], v152\n"
  "v_exp_f32 v68, v68\n"
  "v_exp_f32 v69, v69\n"
  "v_exp_f32 v70, v70\n"
  "v_exp_f32 v71, v71\n"
  "v_mfma_f32_32x32x16_bf16 v[80:95], v[128:131], v[112:115], v[80:95]\n"
  "v_exp_f32 v72, v72\n"
  "v_exp_f32 v73, v73\n"
  "v_exp_f32 v74, v74\n"
  "v_exp_f32 v75, v75\n"
  "v_mfma_f32_32x32x16_bf16 v[96:111], v[128:131], v[116:119], v[96:111]\n"
  "v_exp_f32 v76, v76\n"
  "v_exp_f32 v77, v77\n"
  "v_exp_f32 v78, v78\n"
  "v_exp_f32 v79, v79\n"
  "v_smfmac_f32_32x32x32_bf16 v[32:47], v[136:139], v[64:71], v152\n"
  "s_waitcnt lgkmcnt(0)\n"
  "v_exp_f32 v80, v80\n"
  "v_exp_f32 v81, v81\n"
  "v_exp_f32 v82, v82\n"
  "v_exp_f32 v83, v83\n"
  "v_smfmac_f32_32x32x32_bf16 v[32:47], v[144:147], v[72:79], v152\n"
  "v_exp_f32 v84, v84\n"
  "v_exp_f32 v85, v85\n"
  "v_exp_f32 v86, v86\n"
  "v_exp_f32 v87, v87\n"
  "v_exp_f32 v88, v88\n"
  "v_exp_f32 v89, v89\n"
  "v_exp_f32 v90, v90\n"
  "v_exp_f32 v91, v91\n"
  "v_exp_f32 v92, v92\n"
  "v_exp_f32 v93, v93\n"
  "v_exp_f32 v94, v94\n"
  "v_exp_f32 v95, v95\n"
  "v_smfmac_f32_32x32x32_bf16 v[16:31], v[140:143], v[80:87], v152\n"
  "v_exp_f32 v96, v96\n"
  "v_exp_f32 v97, v97\n"
  "v_exp_f32 v98, v98\n"
  "v_exp_f32 v99, v99\n"
  "v_smfmac_f32_32x32x32_bf16 v[16:31], v[148:151], v[88:95], v152\n"
  "v_exp_f32 v100, v100\n"
  "v_exp_f32 v101, v101\n"
  "v_exp_f32 v102, v102\n"
  "v_exp_f32 v103, v103\n"
  "v_exp_f32 v104, v104\n"
  "v_exp_f32 v105, v105\n"
  "v_exp_f32 v106, v106\n"
  "v_exp_f32 v107, v107\n"
  "v_exp_f32 v108, v108\n"
  "v_exp_f32 v109, v109\n"
  "v_exp_f32 v110, v110\n"
  "v_exp_f32 v111, v111\n"
  "v_smfmac_f32_32x32x32_bf16 v[32:47], v[140:143], v[96:103], v152\n"
  "s_nop 1\n"
  "v_smfmac_f32_32x32x32_bf16 v[32:47], v[148:151], v[104:111], v152\n"
  "s_nop 15\n"
  : "={v[16:31]}"(oa), "={v[32:47]}"(ob)
  : "{v[112:115]}"(qa0), "{v[116:119]}"(qb0), "{v[120:121]}"(q2b), "{v[156:171]}"(na), "{v[172:187]}"(nb), "{v153}"(ak0), "{v154}"(ak1), "{v155}"(av)
  : "memory", "scc", "s20", "v48", "v49", "v50", "v51", "v52", "v53", "v54", "v55", "v56", "v57", "v58", "v59", "v60", "v61", "v62", "v63", "v64", "v65", "v66", "v67", "v68", "v69", "v70", "v71", "v72", "v73", "v74", "v75", "v76", "v77", "v78", "v79", "v80", "v81", "v82", "v83", "v84", "v85", "v86", "v87", "v88", "v89", "v90", "v91", "v92", "v93", "v94", "v95", "v96", "v97", "v98", "v99", "v100", "v101", "v102", "v103", "v104", "v105", "v106", "v107", "v108", "v109", "v110", "v111", "v124", "v125", "v126", "v127", "v128", "v129", "v130", "v131", "v132", "v133", "v134", "v135", "v136", "v137", "v138", "v139", "v140", "v141", "v142", "v143", "v144", "v145", "v146", "v147", "v148", "v149", "v150", "v151", "v152", "v188", "v189", "v190", "v191", "v192", "v193", "v194", "v195");
  BARRIER();
  auto bad = [&](const f32x16& o) {
    f32x2 z = {0.f, 0.f};
#pragma unroll
    for (int i = 0; i < 12; i += 2) z = __builtin_elementwise_fma((f32x2){o[i], o[i + 1]}, (f32x2){0.f, 0.f}, z);
    const float l = h ? o[8] : 1.f;
    return !(z[0] + z[1] == 0.f && l > 1.0e-20f && l < 1.0e30f); };
  const bool ba = bad(oa), bb = bad(ob);
  if (__builtin_expect(__builtin_amdgcn_ballot_w64(ba || bb) != 0, 0)) {
    oa = attn_safe(ws + WS_KV, __builtin_bit_cast(bf16x8, qa0), __builtin_bit_cast(s16x4, qa1), lane);
    ob = attn_safe(ws + WS_KV, __builtin_bit_cast(bf16x8, qb0), __builtin_bit_cast(s16x4, qb1), lane);
  }
  {
    const float xa = xhalf(oa[8]), xb = xhalf(ob[8]);
    const float sa = gate * __builtin_amdgcn_rcpf(h ? oa[8] : xa), sb = gate * __builtin_amdgcn_rcpf(h ? ob[8] : xb);
    const unsigned op = (tok >> 4) * 40960 + hg * 1280 + (tok & 15) * 16;
    st16(oi, op + h * 256, nosent((u32x4){cvtpk(oa[0] * sa, oa[1] * sa), cvtpk(oa[2] * sa, oa[3] * sa), cvtpk(oa[4] * sa, oa[5] * sa), cvtpk(oa[6] * sa, oa[7] * sa)}));
    st16(oi, op + 512 + h * 256, nosent((u32x4){cvtpk(ob[0] * sb, ob[1] * sb), cvtpk(ob[2] * sb, ob[3] * sb), cvtpk(ob[4] * sb, ob[5] * sb), cvtpk(ob[6] * sb, ob[7] * sb)}));
    if (!h) st16(oi, op + 1024, nosent((u32x4){cvtpk(oa[8] * sa, oa[9] * sa), cvtpk(oa[10] * sa, oa[11] * sa), cvtpk(ob[8] * sb, ob[9] * sb), cvtpk(ob[10] * sb, ob[11] * sb)}));
  }
  {
    const int h0 = (hg >> 3) ^ 1;
    const unsigned lo = lane * 16, oa0 = (tq + wid) * 40960 + h0 * 10240, oa1 = (tq + wid) * 40960 + (h0 ^ 1) * 10240;
    const LDSP char* wl0 = L + h0 * 51200 + lo;
    const LDSP char* wl1 = L + (h0 ^ 1) * 51200 + lo;
    u32x4 of[20];
    auto fetch = [&](int i) { return ld16(oi, lo + (i % 10) * 1024, i < 10 ? oa0 : oa1); };
    auto wfr = [&](int f) { return *(const LDSP bf16x8*)((f < 50 ? wl0 : wl1) + (f % 50) * 1024); };
#pragma unroll
    for (int i = 0; i < 10; ++i) of[i] = fetch(i);
    bf16x8 wr[10];
#pragma unroll
    for (int i = 0; i < 10; ++i) wr[i] = wfr(i);
    __builtin_amdgcn_s_sleep(12);
#pragma unroll
    for (int i = 10; i < 20; ++i) of[i] = fetch(i);
    f32x4 acc[5] = {}, bsum[5];
    BARRIER();
    auto marked = [&](int g) { const u32x4 mx = __builtin_elementwise_max(of[g * 2], of[g * 2 + 1]);
      return __builtin_amdgcn_ballot_w64(max(max(mx[0], mx[1]), max(mx[2], mx[3])) == 0xFFFFFFFFu); };
    unsigned long long mk = marked(0);
#pragma unroll
    for (int ks = 0; ks < 20; ++ks) {
      if (ks == 10) {
        BARRIER();
        const int j = (wid + 1) & 3;
#pragma unroll
        for (int ct = 0; ct < 5; ++ct) bsum[ct] = ((const LDSP f32x4*)(L + L_HX) + lane)[(j * 15 + (wid - (wid > j)) * 5 + ct) * 64];
      }
      if ((ks & 1) == 0 && __builtin_expect(mk != 0, 0))
        for (unsigned sp = 0; sp < SPIN_CAP && mk != 0; ++sp) {
          asm volatile("" ::: "memory");
#pragma unroll
          for (int i = ks; i < (ks < 10 ? 10 : 20); ++i) of[i] = fetch(i);
          mk = marked(ks / 2);
        }
#pragma unroll
      for (int ct = 0; ct < 5; ++ct) {
        SBAR();
        acc[ct] = MFMA16(wr[(ks & 1) * 5 + ct], __builtin_bit_cast(bf16x8, of[ks]), acc[ct]);
        SBAR();
        if (ks < 18) wr[(ks & 1) * 5 + ct] = wfr((ks + 2) * 5 + ct);
        if ((ks & 1) && ks < 19 && ct == 1) { mk = marked(ks / 2 + 1); asm volatile("" : "+s"(mk)); }
      }
    }
    u16* op = out + ((tq + wid) * 16 + (lane & 15)) * DM + cgp * 80 + (lane >> 4) * 4;
#pragma unroll
    for (int ct = 0; ct < 5; ++ct) { const f32x4 v = acc[ct] + bsum[ct]; *(u32x2*)(op + ct * 16) = (u32x2){cvtpk(v[0], v[1]), cvtpk(v[2], v[3])}; }
  }
}

extern "C" void kernel_launch(void* const* d_in, const int* in_sizes, int n_in, void* d_out, int out_size, void* d_ws, size_t ws_size, hipStream_t stream) {
  const u16 *x = (const u16*)d_in[0], *wq = (const u16*)d_in[1], *wk = (const u16*)d_in[2], *wv = (const u16*)d_in[3], *woA = (const u16*)d_in[4];
  const u16 *wqkv = (const u16*)d_in[6], *woB = (const u16*)d_in[7], *gw = (const u16*)d_in[8];
  char* ws = (char*)d_ws;
  (void)hipFuncSetAttribute((const void*)k_main, hipFuncAttributeMaxDynamicSharedMemorySize, LDS_MAIN);
  k_main<<<256, 512, LDS_MAIN, stream>>>(x, wq, wk, wv, wqkv, gw, woA, woB, ws, (u16*)d_out);
}
```
